```python
import torch

def _softmax(x, axis=-1):
    return torch.softmax(x, dim=axis)

def _gelu(x):
    return torch.nn.functional.gelu(x, approximate='tanh')

def _silu(x):
    return torch.nn.functional.silu(x)

def _sigmoid(x):
    return torch.sigmoid(x)

def _layernorm(x, axis=-1, eps=1e-05):
    m = x.mean(dim=axis, keepdim=True)
    v = x.var(dim=axis, keepdim=True, unbiased=False)
    return (x - m) / torch.sqrt(v + eps)

def _scatter_add(tgt, idx, src, axis=0):
    return tgt.clone().index_add_(axis, idx, src)

def _segment_sum(src, seg, n):
    out = torch.zeros((n,) + src.shape[1:], dtype=src.dtype)
    return out.index_add_(0, seg, src)

def _pad(x, pw):
    flat = tuple((v for pair in reversed(pw) for v in pair))
    return torch.nn.functional.pad(x, flat)
kernel_name = 'adaLN_cond__grouped_kv_mqa__geglu_ffn__prompt'

def setup_inputs():

    _w = lambda a: (lambda t: t.to(torch.bfloat16) if t.is_floating_point() else t)(torch.as_tensor(a))

    def _A_setup():
        B, S, D, H = (1, 2048, 1024, 32)
        Dh = D // H
        return {'x': _w((lambda *s: torch.randn(*s, dtype=torch.bfloat16) * 0.05)(B, S, D)), 'wq': _w((lambda *s: torch.randn(*s, dtype=torch.bfloat16) * 0.05)(D, H * Dh)), 'wk': _w((lambda *s: torch.randn(*s, dtype=torch.bfloat16) * 0.05)(D, Dh)), 'wv': _w((lambda *s: torch.randn(*s, dtype=torch.bfloat16) * 0.05)(D, Dh)), 'wo': _w((lambda *s: torch.randn(*s, dtype=torch.bfloat16) * 0.05)(H * Dh, D))}
    _A_in = _A_setup()

    def _B_setup():
        B, S, D, F = (1, 2048, 1024, 3584)
        return {'x': _w((lambda *s: torch.randn(*s, dtype=torch.bfloat16) * 0.05)(B, S, D)), 'w_gate': _w((lambda *s: torch.randn(*s, dtype=torch.bfloat16) * 0.05)(D, F)), 'w_up': _w((lambda *s: torch.randn(*s, dtype=torch.bfloat16) * 0.05)(D, F)), 'w_down': _w((lambda *s: torch.randn(*s, dtype=torch.bfloat16) * 0.05)(F, D))}
    _B_in = _B_setup()
    return {'A_x': _A_in['x'], 'A_wq': _A_in['wq'], 'A_wk': _A_in['wk'], 'A_wv': _A_in['wv'], 'A_wo': _A_in['wo'], 'B_x': _B_in['x'], 'B_w_gate': _B_in['w_gate'], 'B_w_up': _B_in['w_up'], 'B_w_down': _B_in['w_down'], 'c': _w((lambda *s: torch.randn(*s, dtype=torch.bfloat16) * 0.1)(1, 1024) * 8.0), 'w_mod': _w((lambda *s: torch.randn(*s, dtype=torch.bfloat16) * 0.1)(1024, 6 * 1024) * 1024 ** (-0.5) * 8.0)}

def reference(A_x, A_wq, A_wk, A_wv, A_wo, B_x, B_w_gate, B_w_up, B_w_down, c, w_mod):

    def _A_ref(x, wq, wk, wv, wo):
        B, S, D = x.shape
        H = 32
        Dh = D // H
        q = (lambda x, *d: x.permute(*d))((x @ wq).reshape(B, S, H, Dh), 0, 2, 1, 3)
        k = x @ wk
        v = x @ wv
        scores = torch.einsum('bhqd,bkd->bhqk', q, k) * Dh ** (-0.5)
        p = _softmax(scores, axis=-1)
        o = torch.einsum('bhqk,bkd->bhqd', p, v)
        return (lambda x, *d: x.permute(*d))(o, 0, 2, 1, 3).reshape(B, S, H * Dh) @ wo

    def _B_ref(x, w_gate, w_up, w_down):
        return _gelu(x @ w_gate) * (x @ w_up) @ w_down
    x = A_x
    mod = (_silu(c) @ w_mod).reshape(c.shape[0], 6, 1, -1)
    sa, ca, ga, sb, cb, gb = (mod[:, 0].clone(), mod[:, 1].clone(), mod[:, 2].clone(), mod[:, 3].clone(), mod[:, 4].clone(), mod[:, 5].clone())
    h = _layernorm(x, axis=-1) * (1 + ca) + sa
    x = x + ga * _A_ref(h, A_wq, A_wk, A_wv, A_wo)
    h = _layernorm(x, axis=-1) * (1 + cb) + sb
    x = x + gb * _B_ref(h, B_w_gate, B_w_up, B_w_down)
    return x
```

```cpp
#include <hip/hip_runtime.h>
#include <stdint.h>

typedef unsigned short bf16_t;
typedef short bf16x8 __attribute__((ext_vector_type(8)));
typedef short s16x4 __attribute__((ext_vector_type(4)));
typedef float f32x2 __attribute__((ext_vector_type(2)));
typedef float f32x4 __attribute__((ext_vector_type(4)));
typedef float f32x16 __attribute__((ext_vector_type(16)));
typedef unsigned u32x2 __attribute__((ext_vector_type(2)));
typedef unsigned u32x4 __attribute__((ext_vector_type(4)));
typedef __bf16 bf16x2_t __attribute__((ext_vector_type(2)));
#define LAS __attribute__((address_space(3)))
typedef LAS const char* lds_cptr;
typedef LAS char* lds_ptr;
#define DI __device__ __forceinline__

constexpr int S_ = 2048, D_ = 1024, F_ = 3584, NMOD = 6144;
constexpr float LN_EPS = 1e-5f;
constexpr float QSCALE = 0.17677669529663687f * 1.4426950408889634f;

constexpr size_t WS_MODP = 0;
constexpr size_t WS_MODF = 128 * 1024;
constexpr size_t WS_WKVF = 192 * 1024;
constexpr size_t WS_KVF  = 384 * 1024;
constexpr size_t WS_UTP  = 640 * 1024;
constexpr size_t WS_STAT = 768 * 1024;
constexpr size_t WS_H    = 1u << 20;
constexpr size_t WS_O    = 5u << 20;
constexpr size_t WS_X1   = 9u << 20;
constexpr size_t WS_Y    = 13u << 20;
constexpr size_t WS_ACT  = 17u << 20;

DI unsigned cvtpk(float lo, float hi) { f32x2 v = {lo, hi}; bf16x2_t b = __builtin_convertvector(v, bf16x2_t); return __builtin_bit_cast(unsigned, b); }
DI float bflo(unsigned u) { return __uint_as_float(u << 16); }
DI float bfhi(unsigned u) { return __uint_as_float(u & 0xffff0000u); }
DI float rbf(float x) { return bflo(cvtpk(x, 0.f) & 0xffffu); }
DI float bf2f(bf16_t b) { return __uint_as_float((unsigned)b << 16); }
DI void glds16(const void* sbase, unsigned voff, unsigned lds_dst) {
  unsigned keep;
  asm volatile("s_mov_b32 %0, m0\n\ts_mov_b32 m0, %3\n\ts_nop 0\n\tglobal_load_lds_dwordx4 %1, %2\n\ts_mov_b32 m0, %0"
               : "=&s"(keep) : "v"(voff), "s"(sbase), "s"(lds_dst) : "memory");
}
#define GLOAD4(dst, sbase, voff, imm) asm volatile("global_load_dwordx4 %0, %1, %2 offset:%3" : "=v"(dst) : "v"(voff), "s"(sbase), "n"(imm) : "memory")
typedef short v4i16_t __attribute__((ext_vector_type(4)));
DI s16x4 ldtr(lds_cptr p) { return __builtin_bit_cast(s16x4, __builtin_amdgcn_ds_read_tr16_b64_v4i16((LAS v4i16_t*)p)); }
DI bf16x8 ldtr2(lds_cptr p0, lds_cptr p1) { s16x4 a = ldtr(p0), b = ldtr(p1); return (bf16x8){a[0], a[1], a[2], a[3], b[0], b[1], b[2], b[3]}; }
DI bf16x8 ld128(lds_cptr p) { return *(const LAS bf16x8*)p; }
#define MFMA32(a, b, c) __builtin_amdgcn_mfma_f32_32x32x16_bf16(a, b, c, 0, 0, 0)
#define MFMA16(a, b, c) __builtin_amdgcn_mfma_f32_16x16x32_bf16(a, b, c, 0, 0, 0)
#define SBAR() __builtin_amdgcn_sched_barrier(0)
#define BAR() asm volatile("s_barrier" ::: "memory")
#define WAITV(n) asm volatile("s_waitcnt vmcnt(" #n ")" ::: "memory")
#define WAITL() asm volatile("s_waitcnt lgkmcnt(0)" ::: "memory")
#define RFL(x) __builtin_amdgcn_readfirstlane(x)

__global__ __launch_bounds__(256) void k1_mod(const bf16_t* __restrict__ c, const bf16_t* __restrict__ wmod, const bf16_t* __restrict__ wk,
                                              const bf16_t* __restrict__ wv, float* __restrict__ modp, bf16_t* __restrict__ wkvf) {
  __shared__ float red[4][64];
  const int b = blockIdx.x, tid = threadIdx.x;
  if (b < 384) {
    const int cg = b % 96, ks = b / 96, l8 = tid & 7, r = tid >> 3;
    u32x4 w[8]; float s[8];
#pragma unroll
    for (int i = 0; i < 8; ++i) { const int k = ks * 256 + i * 32 + r; w[i] = *(const u32x4*)(wmod + (size_t)k * NMOD + cg * 64 + l8 * 8); s[i] = bf2f(c[k]); }
    float acc[8] = {0, 0, 0, 0, 0, 0, 0, 0};
#pragma unroll
    for (int i = 0; i < 8; ++i) {
      const float sv = rbf(s[i] / (1.f + __expf(-s[i])));
#pragma unroll
      for (int j = 0; j < 4; ++j) { acc[2 * j] += sv * bflo(w[i][j]); acc[2 * j + 1] += sv * bfhi(w[i][j]); }
    }
#pragma unroll
    for (int j = 0; j < 8; ++j) { acc[j] += __shfl_xor(acc[j], 8); acc[j] += __shfl_xor(acc[j], 16); acc[j] += __shfl_xor(acc[j], 32); }
    if ((tid & 63) < 8) {
#pragma unroll
      for (int j = 0; j < 8; ++j) red[tid >> 6][l8 * 8 + j] = acc[j];
    }
    __syncthreads();
    if (tid < 64) modp[ks * NMOD + cg * 64 + tid] = (red[0][tid] + red[1][tid]) + (red[2][tid] + red[3][tid]);
  } else {
    const int it = (b - 384) * 256 + tid, lane = it & 63, ct = (it >> 6) & 3, J = it >> 8;
    const bf16_t* src = (ct < 2 ? wk : wv) + (size_t)(32 * J + 8 * (lane >> 4)) * 32 + (ct & 1) * 16 + (lane & 15);
    unsigned o[4];
#pragma unroll
    for (int j = 0; j < 4; ++j) o[j] = (unsigned)src[(2 * j) * 32] | ((unsigned)src[(2 * j + 1) * 32] << 16);
    *(u32x4*)(wkvf + (size_t)it * 8) = (u32x4){o[0], o[1], o[2], o[3]};
  }
}

DI float mod_at(const float* modp, int i) { return rbf((modp[i] + modp[NMOD + i]) + (modp[2 * NMOD + i] + modp[3 * NMOD + i])); }

__global__ __launch_bounds__(512) void k2_ln_kv(const bf16_t* __restrict__ x, const float* __restrict__ modp, const bf16_t* __restrict__ wkvf,
                                                const bf16_t* __restrict__ wg, const bf16_t* __restrict__ wu, bf16_t* __restrict__ hfm,
                                                bf16_t* __restrict__ kvf, float* __restrict__ modf, float* __restrict__ utp) {
  __shared__ __attribute__((aligned(16))) char smem[65536 + 2048];
  const int b = blockIdx.x, tid = threadIdx.x, lane = tid & 63, w = tid >> 6;
  if (b < 128) {
    bf16x8 wf[16];
#pragma unroll
    for (int i = 0; i < 16; ++i) wf[i] = *(const bf16x8*)(wkvf + ((size_t)(w * 16 + i) * 64 + lane) * 8);
    u32x4 xv[2][2];
#pragma unroll
    for (int rr = 0; rr < 2; ++rr)
#pragma unroll
      for (int hh = 0; hh < 2; ++hh) xv[rr][hh] = *(const u32x4*)(x + (size_t)(b * 16 + 2 * w + rr) * D_ + hh * 512 + lane * 8);
    float sh[16], sc[16];
#pragma unroll
    for (int hh = 0; hh < 2; ++hh)
#pragma unroll
      for (int q = 0; q < 2; ++q) {
        const int k = hh * 512 + lane * 8 + q * 4;
        f32x4 a = *(const f32x4*)(modp + k), cc = *(const f32x4*)(modp + 1024 + k);
#pragma unroll
        for (int p = 1; p < 4; ++p) { if (p == 2) continue; }
        const f32x4 a1 = *(const f32x4*)(modp + NMOD + k), a2 = *(const f32x4*)(modp + 2 * NMOD + k), a3 = *(const f32x4*)(modp + 3 * NMOD + k);
        const f32x4 c1 = *(const f32x4*)(modp + NMOD + 1024 + k), c2 = *(const f32x4*)(modp + 2 * NMOD + 1024 + k), c3 = *(const f32x4*)(modp + 3 * NMOD + 1024 + k);
        a = (a + a1) + (a2 + a3); cc = (cc + c1) + (c2 + c3);
#pragma unroll
        for (int j = 0; j < 4; ++j) { sh[hh * 8 + q * 4 + j] = rbf(a[j]); sc[hh * 8 + q * 4 + j] = rbf(1.f + rbf(cc[j])); }
      }
    if (tid < 48) { const int i = b * 48 + tid, sec = i >> 10; float v = mod_at(modp, i); if (sec == 1 || sec == 4) v = rbf(1.f + v); modf[i] = v; }
#pragma unroll
    for (int rr = 0; rr < 2; ++rr) {
      float v[16];
#pragma unroll
      for (int hh = 0; hh < 2; ++hh)
#pragma unroll
        for (int j = 0; j < 4; ++j) { v[hh * 8 + 2 * j] = bflo(xv[rr][hh][j]); v[hh * 8 + 2 * j + 1] = bfhi(xv[rr][hh][j]); }
      float s = 0.f;
#pragma unroll
      for (int j = 0; j < 16; ++j) s += v[j];
#pragma unroll
      for (int o = 1; o < 64; o <<= 1) s += __shfl_xor(s, o);
      const float mean = s * (1.f / D_);
      float q = 0.f;
#pragma unroll
      for (int j = 0; j < 16; ++j) { v[j] -= mean; q += v[j] * v[j]; }
#pragma unroll
      for (int o = 1; o < 64; o <<= 1) q += __shfl_xor(q, o);
      const float rstd = 1.f / sqrtf(q * (1.f / D_) + LN_EPS);
      const int tok = 2 * w + rr;
#pragma unroll
      for (int hh = 0; hh < 2; ++hh) {
        u32x4 o;
#pragma unroll
        for (int j = 0; j < 4; ++j) o[j] = cvtpk(v[hh * 8 + 2 * j] * rstd * sc[hh * 8 + 2 * j] + sh[hh * 8 + 2 * j], v[hh * 8 + 2 * j + 1] * rstd * sc[hh * 8 + 2 * j + 1] + sh[hh * 8 + 2 * j + 1]);
        *(u32x4*)(smem + (hh * 64 + lane) * 272 + tok * 16) = o;
      }
    }
    __syncthreads();
#pragma unroll
    for (int i = 0; i < 4; ++i) {
      const int it = i * 512 + tid, tok = it & 15, ch = it >> 4;
      const u32x4 v = *(const u32x4*)(smem + ch * 272 + tok * 16);
      *(u32x4*)(hfm + ((size_t)((b >> 1) * 64 + (ch >> 1)) * 64 + (b & 1) * 16 + tok + 32 * (ch & 1)) * 8) = v;
    }
    f32x4 acc[4] = {};
#pragma unroll
    for (int jj = 0; jj < 4; ++jj) {
      const bf16x8 a = *(const bf16x8*)(smem + ((w * 4 + jj) * 4 + (lane >> 4)) * 272 + (lane & 15) * 16);
#pragma unroll
      for (int ct = 0; ct < 4; ++ct) acc[ct] = MFMA16(a, wf[jj * 4 + ct], acc[ct]);
    }
    float* red = (float*)(smem + 34816);
#pragma unroll
    for (int ct = 0; ct < 4; ++ct) *(f32x4*)(red + ((w * 4 + ct) * 64 + lane) * 4) = acc[ct];
    __syncthreads();
#pragma unroll
    for (int e = 0; e < 2; ++e) {
      const int it = e * 512 + tid;
      float s = 0.f;
#pragma unroll
      for (int ww = 0; ww < 8; ++ww) s += red[ww * 1024 + it];
      const int r = it & 3, ln = (it >> 2) & 63, ct = it >> 8;
      const int key = b * 16 + 4 * (ln >> 4) + r, d = (ct & 1) * 16 + (ln & 15), kt = key >> 5, kl = key & 31;
      int idx;
      if (ct < 2) { const int dd = d & 15; idx = ((d >> 4) * 64 + kl + 32 * ((dd >> 2) & 1)) * 8 + (((dd >> 3) << 2) | (dd & 3)); }
      else { const int kk = kl & 15, rho = (d & 3) | (((d >> 3) & 1) << 2) | (((d >> 4) & 1) << 3) | (((d >> 2) & 1) << 4);
             idx = ((2 + (kl >> 4)) * 64 + rho + 32 * ((kk >> 2) & 1)) * 8 + (((kk >> 3) << 2) | (kk & 3)); }
      kvf[(size_t)kt * 2048 + idx] = (bf16_t)(cvtpk(s, 0.f) & 0xffffu);
    }
  } else {
    const int bb = b - 128, cg = bb % 112, ks = bb / 112, l8 = tid & 7, r = tid >> 3;
    const bf16_t* W = (cg < 56 ? wg + cg * 64 : wu + (cg - 56) * 64) + l8 * 8;
    u32x4 wv[8]; float cu[8], ct[8];
#pragma unroll
    for (int i = 0; i < 8; ++i) { const int k = ks * 512 + i * 64 + r; wv[i] = *(const u32x4*)(W + (size_t)k * F_); cu[i] = rbf(1.f + mod_at(modp, 4096 + k)); ct[i] = mod_at(modp, 3072 + k); }
    float au[8] = {0, 0, 0, 0, 0, 0, 0, 0}, at[8] = {0, 0, 0, 0, 0, 0, 0, 0};
#pragma unroll
    for (int i = 0; i < 8; ++i)
#pragma unroll
      for (int j = 0; j < 4; ++j) { const float lo = bflo(wv[i][j]), hi = bfhi(wv[i][j]); au[2 * j] += cu[i] * lo; au[2 * j + 1] += cu[i] * hi; at[2 * j] += ct[i] * lo; at[2 * j + 1] += ct[i] * hi; }
#pragma unroll
    for (int j = 0; j < 8; ++j) {
      au[j] += __shfl_xor(au[j], 8); au[j] += __shfl_xor(au[j], 16); au[j] += __shfl_xor(au[j], 32);
      at[j] += __shfl_xor(at[j], 8); at[j] += __shfl_xor(at[j], 16); at[j] += __shfl_xor(at[j], 32);
    }
    float* red = (float*)smem;
    if (lane < 8) {
#pragma unroll
      for (int j = 0; j < 8; ++j) { red[w * 128 + l8 * 8 + j] = au[j]; red[w * 128 + 64 + l8 * 8 + j] = at[j]; }
    }
    __syncthreads();
    if (tid < 128) { float s = 0.f;
#pragma unroll
      for (int ww = 0; ww < 8; ++ww) s += red[ww * 128 + tid];
      utp[(ks * 2 + (tid >> 6)) * 7168 + cg * 64 + (tid & 63)] = s; }
  }
}

DI bf16x8 pack8(const f32x16& p, int s) {
  u32x4 r = {cvtpk(p[8 * s], p[8 * s + 1]), cvtpk(p[8 * s + 2], p[8 * s + 3]), cvtpk(p[8 * s + 4], p[8 * s + 5]), cvtpk(p[8 * s + 6], p[8 * s + 7])};
  return __builtin_bit_cast(bf16x8, r);
}
DI float rowmax16(const f32x16& p) {
  float a = fmaxf(fmaxf(p[0], p[1]), p[2]), b = fmaxf(fmaxf(p[3], p[4]), p[5]);
  a = fmaxf(fmaxf(a, p[6]), p[7]); b = fmaxf(fmaxf(b, p[8]), p[9]); a = fmaxf(fmaxf(a, p[10]), p[11]); b = fmaxf(fmaxf(b, p[12]), p[13]);
  a = fmaxf(fmaxf(a, p[14]), p[15]); a = fmaxf(a, b);
  auto rr = __builtin_amdgcn_permlane32_swap(__float_as_uint(a), __float_as_uint(a), false, false);
  return fmaxf(__uint_as_float(rr[0]), __uint_as_float(rr[1]));
}

__global__ __launch_bounds__(512, 2) void k3_attn(const bf16_t* hfm, const bf16_t* wq, const bf16_t* kvf, bf16_t* ofm) {
  extern __shared__ __attribute__((aligned(16))) char lds[];
  const int tid = threadIdx.x, lane = tid & 63, w = RFL(tid >> 6), wm = w & 3, wk = w >> 2;
  const int xcd = blockIdx.x & 7, bi = blockIdx.x >> 3, tm = (xcd >> 1) * 4 + (bi & 3), hp = (xcd & 1) * 8 + (bi >> 2);
  const unsigned lds0 = (unsigned)(uintptr_t)lds;
  const lds_cptr L = (lds_cptr)lds;
  const int hi = lane >> 5;
  {
    const int r8 = lane >> 3;
    const unsigned voff = (unsigned)((w * 8 + r8) * 2048 + hp * 128 + (((lane & 7) ^ (((r8 >> 1) & 1) << 2)) << 4));
#pragma unroll
    for (int q = 0; q < 16; ++q) glds16((const char*)wq + q * 64 * 2048, voff, lds0 + 32768 + (q * 8 + w) * 1024);
  }
  const unsigned kvoff = (unsigned)(w * 4096 + lane * 16);
#define KV_ISSUE(c, slot) do { _Pragma("unroll") for (int q_ = 0; q_ < 4; ++q_) glds16((const char*)kvf + (c) * 32768 + q_ * 1024, kvoff, lds0 + (slot) * 32768 + w * 4096 + q_ * 1024); } while (0)
  KV_ISSUE(0, 0);
  bf16x8 hf[32];
  {
    const bf16_t* hp_ = hfm + ((size_t)((tm * 4 + wm) * 64 + wk * 32) * 64 + lane) * 8;
#pragma unroll
    for (int i = 0; i < 32; ++i) hf[i] = *(const bf16x8*)(hp_ + i * 512);
  }
  SBAR();
  asm volatile("s_waitcnt vmcnt(32)\n\ts_barrier" ::: "memory");
  f32x16 qa0 = {}, qa1 = {};
  {
    const int q = (lane & 15) >> 2, pp = lane & 3, nb = (lane >> 4) & 1;
    const lds_cptr a0 = L + 32768 + wk * 65536 + hi * 1024 + q * 128 + (((q >> 1) & 1) * 64) + nb * 32 + pp * 8;
    const lds_cptr a1 = L + 32768 + wk * 65536 + hi * 1024 + q * 128 + ((1 ^ ((q >> 1) & 1)) * 64) + nb * 32 + pp * 8;
#pragma unroll
    for (int i = 0; i < 32; ++i) {
      qa0 = MFMA32(ldtr2(a0 + i * 2048, a0 + i * 2048 + 512), hf[i], qa0);
      qa1 = MFMA32(ldtr2(a1 + i * 2048, a1 + i * 2048 + 512), hf[i], qa1);
    }
  }
  asm volatile("s_waitcnt lgkmcnt(0)\n\ts_barrier" ::: "memory");
  KV_ISSUE(1, 1); KV_ISSUE(2, 2); KV_ISSUE(3, 3);
  bf16x8 qf0, qf1;
  {
    const f32x16 snd = wk ? qa0 : qa1; f32x16 kp = wk ? qa1 : qa0;
    lds_ptr xb = (lds_ptr)lds + 4 * 32768;
#pragma unroll
    for (int i = 0; i < 4; ++i) *(LAS f32x4*)(xb + w * 4096 + i * 1024 + lane * 16) = (f32x4){snd[4 * i], snd[4 * i + 1], snd[4 * i + 2], snd[4 * i + 3]};
    asm volatile("s_waitcnt lgkmcnt(0)\n\ts_barrier" ::: "memory");
#pragma unroll
    for (int i = 0; i < 4; ++i) { const f32x4 o = *(const LAS f32x4*)(xb + (w ^ 4) * 4096 + i * 1024 + lane * 16);
#pragma unroll
      for (int j = 0; j < 4; ++j) kp[4 * i + j] = (kp[4 * i + j] + o[j]) * QSCALE; }
    qf0 = pack8(kp, 0); qf1 = pack8(kp, 1);
  }
  bf16x8 sel;
  { const short one = ((lane & 3) == 0 && (((lane >> 2) ^ (lane >> 4)) & 1) == 0) ? (short)0x3f80 : (short)0;
#pragma unroll
    for (int j = 0; j < 8; ++j) sel[j] = one; }
  f32x16 oacc = {}; f32x4 lacc = {};
  const lds_cptr kv0 = L + lane * 16;
  f32x16 sA, sB, negm;
  {
    const f32x16 z = {};
    sA = MFMA32(ld128(kv0), qf0, z); sA = MFMA32(ld128(kv0 + 1024), qf1, sA);
    const float m = rowmax16(sA);
#pragma unroll
    for (int r = 0; r < 16; ++r) { sA[r] -= m; negm[r] = -m; }
  }
#define TILE(SC, SN, pc, pn) do { \
    SN = MFMA32(ld128(pn), qf0, negm); SN = MFMA32(ld128((pn) + 1024), qf1, SN); \
    const bf16x8 v0_ = ld128((pc) + 2048), v1_ = ld128((pc) + 3072); \
    _Pragma("unroll") for (int r = 0; r < 16; ++r) SC[r] = __builtin_amdgcn_exp2f(SC[r]); \
    const bf16x8 p0_ = pack8(SC, 0), p1_ = pack8(SC, 1); \
    oacc = MFMA32(v0_, p0_, oacc); lacc = MFMA16(sel, p0_, lacc); \
    oacc = MFMA32(v1_, p1_, oacc); lacc = MFMA16(sel, p1_, lacc); } while (0)
  int slot = 0;
  for (int c = 0; c < 8; ++c) {
    const int nslot = slot == 4 ? 0 : slot + 1;
    const lds_cptr pc = kv0 + slot * 32768;
    const lds_cptr pn = c < 7 ? kv0 + nslot * 32768 : pc;
    TILE(sA, sB, pc, pc + 4096);
    TILE(sB, sA, pc + 4096, pc + 2 * 4096);
    TILE(sA, sB, pc + 2 * 4096, pc + 3 * 4096);
    TILE(sB, sA, pc + 3 * 4096, pc + 4 * 4096);
    TILE(sA, sB, pc + 4 * 4096, pc + 5 * 4096);
    TILE(sB, sA, pc + 5 * 4096, pc + 6 * 4096);
    TILE(sA, sB, pc + 6 * 4096, pc + 7 * 4096);
    asm volatile("s_waitcnt vmcnt(0) lgkmcnt(0)\n\ts_barrier" ::: "memory");
    if (c < 4) { const int fs = c == 0 ? 4 : c - 1; KV_ISSUE(c + 4, fs); }
    TILE(sB, sA, pc + 7 * 4096, pn);
    slot = nslot;
  }
  float lsum = lacc[0];
  bool bad = !(lsum < 1.0e12f);
#pragma unroll
  for (int r = 0; r < 16; ++r) bad = bad || !(fabsf(oacc[r]) < 3.0e38f);
  if (__any(bad)) {
    float m = -INFINITY; lsum = 0.f;
#pragma unroll
    for (int r = 0; r < 16; ++r) oacc[r] = 0.f;
    const bf16_t* g = kvf + lane * 8;
    for (int t = 0; t < 64; ++t) {
      const f32x16 z = {};
      f32x16 s = MFMA32(*(const bf16x8*)(g + t * 2048), qf0, z); s = MFMA32(*(const bf16x8*)(g + t * 2048 + 512), qf1, s);
      const float mn = fmaxf(m, rowmax16(s)), al = __builtin_amdgcn_exp2f(m - mn); m = mn;
#pragma unroll
      for (int r = 0; r < 16; ++r) { s[r] = __builtin_amdgcn_exp2f(s[r] - mn); oacc[r] *= al; }
      const bf16x8 p0 = pack8(s, 0), p1 = pack8(s, 1);
      f32x4 la = {}; la = MFMA16(sel, p0, la); la = MFMA16(sel, p1, la);
      lsum = lsum * al + la[0];
      oacc = MFMA32(*(const bf16x8*)(g + t * 2048 + 1024), p0, oacc); oacc = MFMA32(*(const bf16x8*)(g + t * 2048 + 1536), p1, oacc);
    }
  }
  const float rl = 1.f / lsum;
#pragma unroll
  for (int r = 0; r < 16; ++r) oacc[r] *= rl;
  {
    const int m16 = tm * 8 + wm * 2 + ((lane >> 4) & 1), J = hp * 2 + wk;
    bf16_t* op = ofm + ((size_t)(m16 * 32 + J) * 64 + (lane & 15) + 32 * hi) * 8;
    *(bf16x8*)op = pack8(oacc, 0); *(bf16x8*)(op + 128) = pack8(oacc, 1);
  }
}

DI unsigned wimg_src_chunk(int lane) { return (unsigned)(((lane & 7) ^ (((lane >> 4) & 3) << 1)) << 4); }
DI int wimg_rd(int lane, int t) { const int g = lane >> 4, q = (lane & 15) >> 2, r8 = 4 * (g & 1) + q; return (g >> 1) * 1024 + r8 * 128 + ((t ^ (r8 >> 1)) * 32) + (lane & 3) * 8; }

__global__ __launch_bounds__(512, 2) void k4_oproj(const bf16_t* ofm, const bf16_t* wo, const bf16_t* x, const float* modf, bf16_t* x1fm, bf16_t* yfm, f32x2* stat) {
  extern __shared__ __attribute__((aligned(16))) char lds[];
  const int tid = threadIdx.x, lane = tid & 63, w = RFL(tid >> 6), wm = w & 3, wk = w >> 2;
  const int xcd = blockIdx.x & 7, bi = blockIdx.x >> 3, tm = (xcd >> 1) * 4 + (bi & 3), tn = (xcd & 1) * 8 + (bi >> 2);
  const unsigned lds0 = (unsigned)(uintptr_t)lds;
  const lds_cptr L = (lds_cptr)lds;
  {
    const unsigned voff = (unsigned)((w * 8 + (lane >> 3)) * 2048 + tn * 128) + wimg_src_chunk(lane);
#pragma unroll
    for (int q = 0; q < 16; ++q) glds16((const char*)wo + q * 64 * 2048, voff, lds0 + (q * 8 + w) * 1024);
  }
  bf16x8 af[16][2];
  {
    const bf16_t* ap = ofm + ((size_t)((tm * 8 + wm * 2) * 32 + wk * 16) * 64 + lane) * 8;
#pragma unroll
    for (int i = 0; i < 16; ++i) { af[i][0] = *(const bf16x8*)(ap + i * 512); af[i][1] = *(const bf16x8*)(ap + 32 * 512 + i * 512); }
  }
  const int g = lane >> 4, tok = tm * 128 + wm * 32 + wk * 16 + (lane & 15), col0 = tn * 64 + 4 * g;
  u32x2 xr[4]; f32x4 gav[4], cbv[4];
#pragma unroll
  for (int t = 0; t < 4; ++t) { xr[t] = *(const u32x2*)(x + (size_t)tok * D_ + col0 + 16 * t); gav[t] = *(const f32x4*)(modf + 2048 + col0 + 16 * t); cbv[t] = *(const f32x4*)(modf + 4096 + col0 + 16 * t); }
  SBAR();
  asm volatile("s_waitcnt vmcnt(44)\n\ts_barrier" ::: "memory");
  f32x4 acc[2][4] = {};
  {
    lds_cptr a[4];
#pragma unroll
    for (int t = 0; t < 4; ++t) a[t] = L + wk * 65536 + wimg_rd(lane, t);
#pragma unroll
    for (int i = 0; i < 16; ++i)
#pragma unroll
      for (int t = 0; t < 4; ++t) {
        const bf16x8 wf = ldtr2(a[t] + i * 4096, a[t] + i * 4096 + 2048);
        acc[0][t] = MFMA16(wf, af[i][0], acc[0][t]); acc[1][t] = MFMA16(wf, af[i][1], acc[1][t]);
      }
  }
  lds_ptr xb = (lds_ptr)lds + 131072;
#pragma unroll
  for (int t = 0; t < 4; ++t) *(LAS f32x4*)(xb + w * 4096 + t * 1024 + lane * 16) = wk ? acc[0][t] : acc[1][t];
  asm volatile("s_waitcnt lgkmcnt(0)\n\ts_barrier" ::: "memory");
  float v[16]; float s = 0.f;
#pragma unroll
  for (int t = 0; t < 4; ++t) {
    const f32x4 o = *(const LAS f32x4*)(xb + (w ^ 4) * 4096 + t * 1024 + lane * 16) + (wk ? acc[1][t] : acc[0][t]);
    v[4 * t] = rbf(bflo(xr[t][0]) + gav[t][0] * o[0]); v[4 * t + 1] = rbf(bfhi(xr[t][0]) + gav[t][1] * o[1]);
    v[4 * t + 2] = rbf(bflo(xr[t][1]) + gav[t][2] * o[2]); v[4 * t + 3] = rbf(bfhi(xr[t][1]) + gav[t][3] * o[3]);
    s += (v[4 * t] + v[4 * t + 1]) + (v[4 * t + 2] + v[4 * t + 3]);
  }
  s += __shfl_xor(s, 16); s += __shfl_xor(s, 32);
  const float mw = s * (1.f / 64.f); float q = 0.f;
#pragma unroll
  for (int j = 0; j < 16; ++j) { const float d = v[j] - mw; q += d * d; }
  q += __shfl_xor(q, 16); q += __shfl_xor(q, 32);
  if (g == 0) stat[tok * 16 + tn] = (f32x2){mw, q};
  const size_t fo = ((size_t)((tok >> 4) * 32 + tn * 2) * 64 + lane) * 8;
#pragma unroll
  for (int jj = 0; jj < 2; ++jj) {
    const float* p = v + 8 * jj; const f32x4 c0 = cbv[2 * jj], c1 = cbv[2 * jj + 1];
    *(u32x4*)(x1fm + fo + jj * 512) = (u32x4){cvtpk(p[0], p[1]), cvtpk(p[2], p[3]), cvtpk(p[4], p[5]), cvtpk(p[6], p[7])};
    *(u32x4*)(yfm + fo + jj * 512) = (u32x4){cvtpk(p[0] * c0[0], p[1] * c0[1]), cvtpk(p[2] * c0[2], p[3] * c0[3]), cvtpk(p[4] * c1[0], p[5] * c1[1]), cvtpk(p[6] * c1[2], p[7] * c1[3])};
  }
}

DI float gelu_mul(float g, float u) {
  const float z = g * (g * g * (-0.044715f * 2.302208198f) + (-2.302208198f));
  return g * u * __builtin_amdgcn_rcpf(1.f + __builtin_amdgcn_exp2f(z));
}
__global__ __launch_bounds__(512, 2) void k5_gateup(const bf16_t* yfm, const bf16_t* wg, const bf16_t* wu, const f32x2* stat, const float* utp, bf16_t* actfm) {
  extern __shared__ __attribute__((aligned(16))) char lds[];
  const int tid = threadIdx.x, lane = tid & 63, w = RFL(tid >> 6), wm = w & 3, wn = w >> 2;
  const int xcd = blockIdx.x & 7, bi = blockIdx.x >> 3, tm = (xcd & 1) * 4 + (bi & 3), tn = (xcd >> 1) * 7 + (bi >> 2);
  const unsigned lds0 = (unsigned)(uintptr_t)lds;
  const lds_cptr L = (lds_cptr)lds;
  u32x4 st[8];
  {
    const unsigned so = tid < 256 ? (unsigned)((tm * 256 + tid) * 128) : 0u;
#pragma unroll
    for (int i = 0; i < 8; ++i) GLOAD4(st[i], stat, so, i * 16);
  }
  u32x4 utv[2];
  {
    const int e = (tid - 256) & 127, a = e >> 5;
    const unsigned uo = (unsigned)(((a & 1) * 7168 + (a >> 1) * 3584 + tn * 128 + (e & 31) * 4) * 4);
    GLOAD4(utv[0], utp, uo, 0);
    GLOAD4(utv[1], (const char*)utp + 2 * 7168 * 4, uo, 0);
  }
  const char* sA = (const char*)yfm + (size_t)(tm * 16 + 2 * w) * 32768;
  const char* sB = (const char*)(w < 4 ? wg : wu) + (size_t)(tn * 128 + ((w >> 1) & 1) * 64) * 2 + (size_t)(16 * (w & 1)) * 7168;
  const unsigned vA = (unsigned)(lane * 16), vB = (unsigned)((lane >> 3) * 7168) + wimg_src_chunk(lane);
#define K5_ISSUE(s, slot) do { const unsigned d_ = lds0 + (slot) * 32768 + w * 2048; \
    glds16(sA + (s) * 1024, vA, d_); glds16(sA + (s) * 1024 + 32768, vA, d_ + 1024); \
    glds16(sB + (size_t)(s) * 32 * 7168, vB, d_ + 16384); glds16(sB + (size_t)(s) * 32 * 7168 + 8 * 7168, vB, d_ + 16384 + 1024); } while (0)
  K5_ISSUE(0, 0); K5_ISSUE(1, 1); K5_ISSUE(2, 2);
  asm volatile("s_waitcnt vmcnt(12)" : "+v"(st[0]), "+v"(st[1]), "+v"(st[2]), "+v"(st[3]), "+v"(st[4]), "+v"(st[5]), "+v"(st[6]), "+v"(st[7]), "+v"(utv[0]), "+v"(utv[1]) :: "memory");
  LAS float* tabs = (LAS float*)((lds_ptr)lds + 131072);
  if (tid < 256) {
    float ms = 0.f;
#pragma unroll
    for (int i = 0; i < 8; ++i) ms += __uint_as_float(st[i][0]) + __uint_as_float(st[i][2]);
    const float mean = ms * (1.f / 16.f); float q = 0.f;
#pragma unroll
    for (int i = 0; i < 8; ++i) { const float d0 = __uint_as_float(st[i][0]) - mean, d1 = __uint_as_float(st[i][2]) - mean;
      q += (__uint_as_float(st[i][1]) + __uint_as_float(st[i][3])) + 64.f * (d0 * d0 + d1 * d1); }
    const float rstd = 1.f / sqrtf(q * (1.f / D_) + LN_EPS);
    *(LAS f32x2*)(tabs + tid * 2) = (f32x2){rstd, rstd * mean};
  } else if (tid < 384) {
    *(LAS f32x4*)(tabs + 512 + (tid - 256) * 4) = __builtin_bit_cast(f32x4, utv[0]) + __builtin_bit_cast(f32x4, utv[1]);
  }
  f32x4 ag[4][4] = {}, au[4][4] = {};
  const lds_cptr ra = L + wm * 4096 + lane * 16;
  lds_cptr rb[4];
#pragma unroll
  for (int t = 0; t < 4; ++t) rb[t] = L + 16384 + wn * 4096 + wimg_rd(lane, t);
  asm volatile("s_waitcnt vmcnt(8) lgkmcnt(0)\n\ts_barrier" ::: "memory");
  if (wn) BAR();
#define K5_STEP(slot, ISSUE, WN) do { \
    bf16x8 xa[4], wgf[4], wuf[4]; \
    _Pragma("unroll") for (int t = 0; t < 4; ++t) { wgf[t] = ldtr2(rb[t] + (slot) * 32768, rb[t] + (slot) * 32768 + 2048); wuf[t] = ldtr2(rb[t] + (slot) * 32768 + 8192, rb[t] + (slot) * 32768 + 8192 + 2048); } \
    _Pragma("unroll") for (int m = 0; m < 4; ++m) xa[m] = ld128(ra + (slot) * 32768 + m * 1024); \
    ISSUE; \
    asm volatile("s_waitcnt vmcnt(" #WN ") lgkmcnt(0)\n\ts_barrier" ::: "memory"); SBAR(); \
    __builtin_amdgcn_s_setprio(1); \
    _Pragma("unroll") for (int m = 0; m < 4; ++m) _Pragma("unroll") for (int t = 0; t < 4; ++t) { ag[m][t] = MFMA16(wgf[t], xa[m], ag[m][t]); au[m][t] = MFMA16(wuf[t], xa[m], au[m][t]); } \
    __builtin_amdgcn_s_setprio(0); SBAR(); \
    BAR(); } while (0)
  for (int s = 0; s < 28; s += 4) {
    K5_STEP(0, K5_ISSUE(s + 3, 3), 8);
    K5_STEP(1, K5_ISSUE(s + 4, 0), 8);
    K5_STEP(2, K5_ISSUE(s + 5, 1), 8);
    K5_STEP(3, K5_ISSUE(s + 6, 2), 8);
  }
  K5_STEP(0, K5_ISSUE(31, 3), 8);
  K5_STEP(1, (void)0, 4);
  K5_STEP(2, (void)0, 0);
  K5_STEP(3, (void)0, 0);
  if (!wn) BAR();
  const int g = lane >> 4;
#pragma unroll
  for (int jj = 0; jj < 2; ++jj) {
    f32x4 ug[2], tg[2], uu[2], tu[2];
#pragma unroll
    for (int tt = 0; tt < 2; ++tt) { const int cidx = wn * 64 + (2 * jj + tt) * 16 + 4 * g;
      ug[tt] = *(const LAS f32x4*)(tabs + 512 + cidx); tg[tt] = *(const LAS f32x4*)(tabs + 512 + 128 + cidx);
      uu[tt] = *(const LAS f32x4*)(tabs + 512 + 256 + cidx); tu[tt] = *(const LAS f32x4*)(tabs + 512 + 384 + cidx); }
#pragma unroll
    for (int m = 0; m < 4; ++m) {
      const f32x2 rs = *(const LAS f32x2*)(tabs + (wm * 64 + m * 16 + (lane & 15)) * 2);
      float o[8];
#pragma unroll
      for (int tt = 0; tt < 2; ++tt)
#pragma unroll
        for (int r = 0; r < 4; ++r) {
          const float gg = rs[0] * ag[m][2 * jj + tt][r] + (tg[tt][r] - rs[1] * ug[tt][r]);
          const float uv = rs[0] * au[m][2 * jj + tt][r] + (tu[tt][r] - rs[1] * uu[tt][r]);
          o[4 * tt + r] = gelu_mul(gg, uv);
        }
      *(u32x4*)(actfm + ((size_t)((tm * 16 + wm * 4 + m) * 112 + tn * 4 + wn * 2 + jj) * 64 + lane) * 8) = (u32x4){cvtpk(o[0], o[1]), cvtpk(o[2], o[3]), cvtpk(o[4], o[5]), cvtpk(o[6], o[7])};
    }
  }
}

__global__ __launch_bounds__(512, 2) void k6_down(const bf16_t* actfm, const bf16_t* wd, const bf16_t* x1fm, const float* modf, bf16_t* out) {
  extern __shared__ __attribute__((aligned(16))) char lds[];
  const int tid = threadIdx.x, lane = tid & 63, w = RFL(tid >> 6), wm = w & 3, wk = w >> 2;
  const int xcd = blockIdx.x & 7, bi = blockIdx.x >> 3, tm = (xcd >> 1) * 4 + (bi & 3), tn = (xcd & 1) * 8 + (bi >> 2);
  const unsigned lds0 = (unsigned)(uintptr_t)lds;
  const lds_cptr L = (lds_cptr)lds;
  const char* sW = (const char*)wd + (size_t)(16 * w) * 2048 + tn * 128;
  const unsigned vW = (unsigned)((lane >> 3) * 2048) + wimg_src_chunk(lane);
  const char* sA0 = (const char*)actfm + ((size_t)(tm * 8 + wm * 2) * 112 + 2 * wk) * 1024;
  const char* sA1 = sA0 + 112 * 1024;
  const unsigned vA = (unsigned)(lane * 16);
  u32x4 ar[4][4];
#define K6_ISSUE(st, u, slot) do { const unsigned d_ = lds0 + (slot) * 16384 + w * 2048; \
    glds16(sW + (size_t)(st) * 128 * 2048, vW, d_); glds16(sW + (size_t)(st) * 128 * 2048 + 8 * 2048, vW, d_ + 1024); \
    GLOAD4(ar[u][0], sA0 + (st) * 4096, vA, 0); GLOAD4(ar[u][1], sA1 + (st) * 4096, vA, 0); GLOAD4(ar[u][2], sA0 + (st) * 4096, vA, 1024); GLOAD4(ar[u][3], sA1 + (st) * 4096, vA, 1024); } while (0)
  K6_ISSUE(0, 0, 0); K6_ISSUE(1, 1, 1); K6_ISSUE(2, 2, 2); K6_ISSUE(3, 3, 3);
  f32x4 acc[2][4] = {};
  lds_cptr a[4];
#pragma unroll
  for (int t = 0; t < 4; ++t) a[t] = L + wk * 8192 + wimg_rd(lane, t);
#define K6_STEP(u, half, WN, ISSUE) do { \
    asm volatile("s_waitcnt vmcnt(" #WN ")\n\ts_barrier" : "+v"(ar[u][0]), "+v"(ar[u][1]), "+v"(ar[u][2]), "+v"(ar[u][3]) :: "memory"); \
    _Pragma("unroll") for (int i = 0; i < 2; ++i) _Pragma("unroll") for (int t = 0; t < 4; ++t) { \
      const bf16x8 wf = ldtr2(a[t] + (half) + (u) * 16384 + i * 4096, a[t] + (half) + (u) * 16384 + i * 4096 + 2048); \
      acc[0][t] = MFMA16(wf, __builtin_bit_cast(bf16x8, ar[u][2 * i]), acc[0][t]); acc[1][t] = MFMA16(wf, __builtin_bit_cast(bf16x8, ar[u][2 * i + 1]), acc[1][t]); } \
    SBAR(); ISSUE; } while (0)
  int half = 0;
  for (int s = 0; s < 24; s += 4) {
    const int nh = (half ^ 65536) >> 14;
    K6_STEP(0, half, 18, K6_ISSUE(s + 4, 0, nh + 0));
    K6_STEP(1, half, 18, K6_ISSUE(s + 5, 1, nh + 1));
    K6_STEP(2, half, 18, K6_ISSUE(s + 6, 2, nh + 2));
    K6_STEP(3, half, 18, K6_ISSUE(s + 7, 3, nh + 3));
    half ^= 65536;
  }
  const int g = lane >> 4, tok = tm * 128 + wm * 32 + wk * 16 + (lane & 15), col0 = tn * 64 + 4 * g;
  K6_STEP(0, half, 18, (void)0);
  K6_STEP(1, half, 12, (void)0);
  K6_STEP(2, half, 6, (void)0);
  K6_STEP(3, half, 0, (void)0);
  u32x4 xr[2]; f32x4 gbv[4];
  { const size_t fo = ((size_t)((tok >> 4) * 32 + tn * 2) * 64 + lane) * 8;
    xr[0] = *(const u32x4*)(x1fm + fo); xr[1] = *(const u32x4*)(x1fm + fo + 512);
#pragma unroll
    for (int t = 0; t < 4; ++t) gbv[t] = *(const f32x4*)(modf + 5120 + col0 + 16 * t); }
  lds_ptr xb = (lds_ptr)lds + 131072;
#pragma unroll
  for (int t = 0; t < 4; ++t) *(LAS f32x4*)(xb + w * 4096 + t * 1024 + lane * 16) = wk ? acc[0][t] : acc[1][t];
  asm volatile("s_waitcnt lgkmcnt(0)\n\ts_barrier" ::: "memory");
#pragma unroll
  for (int jj = 0; jj < 2; ++jj) {
    unsigned pk[2][2];
#pragma unroll
    for (int tt = 0; tt < 2; ++tt) { const int t = 2 * jj + tt;
      const f32x4 o = *(const LAS f32x4*)(xb + (w ^ 4) * 4096 + t * 1024 + lane * 16) + (wk ? acc[1][t] : acc[0][t]);
      pk[tt][0] = cvtpk(bflo(xr[jj][2 * tt]) + gbv[t][0] * o[0], bfhi(xr[jj][2 * tt]) + gbv[t][1] * o[1]);
      pk[tt][1] = cvtpk(bflo(xr[jj][2 * tt + 1]) + gbv[t][2] * o[2], bfhi(xr[jj][2 * tt + 1]) + gbv[t][3] * o[3]); }
    auto r0 = __builtin_amdgcn_permlane16_swap(pk[0][0], pk[1][0], false, false);
    auto r1 = __builtin_amdgcn_permlane16_swap(pk[0][1], pk[1][1], false, false);
    *(u32x4*)(out + (size_t)tok * D_ + tn * 64 + jj * 32 + (g & 1) * 16 + (g >> 1) * 8) = (u32x4){r0[0], r1[0], r0[1], r1[1]};
  }
}

extern "C" void kernel_launch(void* const* d_in, const int* in_sizes, int n_in, void* d_out, int out_size, void* d_ws, size_t ws_size, hipStream_t stream) {
  const bf16_t* x = (const bf16_t*)d_in[0]; const bf16_t* wq = (const bf16_t*)d_in[1]; const bf16_t* wk = (const bf16_t*)d_in[2];
  const bf16_t* wv = (const bf16_t*)d_in[3]; const bf16_t* wo = (const bf16_t*)d_in[4]; const bf16_t* wg = (const bf16_t*)d_in[6];
  const bf16_t* wu = (const bf16_t*)d_in[7]; const bf16_t* wd = (const bf16_t*)d_in[8]; const bf16_t* c = (const bf16_t*)d_in[9];
  const bf16_t* wmod = (const bf16_t*)d_in[10];
  char* ws = (char*)d_ws;
  float* modp = (float*)(ws + WS_MODP); float* modf = (float*)(ws + WS_MODF); bf16_t* wkvf = (bf16_t*)(ws + WS_WKVF); bf16_t* kvf = (bf16_t*)(ws + WS_KVF);
  float* utp = (float*)(ws + WS_UTP); f32x2* stat = (f32x2*)(ws + WS_STAT);
  bf16_t* hfm = (bf16_t*)(ws + WS_H); bf16_t* ofm = (bf16_t*)(ws + WS_O); bf16_t* x1fm = (bf16_t*)(ws + WS_X1); bf16_t* yfm = (bf16_t*)(ws + WS_Y); bf16_t* actfm = (bf16_t*)(ws + WS_ACT);
  (void)hipFuncSetAttribute((const void*)k3_attn, hipFuncAttributeMaxDynamicSharedMemorySize, 163840);
  (void)hipFuncSetAttribute((const void*)k4_oproj, hipFuncAttributeMaxDynamicSharedMemorySize, 163840);
  (void)hipFuncSetAttribute((const void*)k5_gateup, hipFuncAttributeMaxDynamicSharedMemorySize, 135168);
  (void)hipFuncSetAttribute((const void*)k6_down, hipFuncAttributeMaxDynamicSharedMemorySize, 163840);
  k1_mod<<<416, 256, 0, stream>>>(c, wmod, wk, wv, modp, wkvf);
  k2_ln_kv<<<352, 512, 0, stream>>>(x, modp, wkvf, wg, wu, hfm, kvf, modf, utp);
  k3_attn<<<256, 512, 163840, stream>>>(hfm, wq, kvf, ofm);
  k4_oproj<<<256, 512, 163840, stream>>>(ofm, wo, x, modf, x1fm, yfm, stat);
  k5_gateup<<<224, 512, 135168, stream>>>(yfm, wg, wu, stat, utp, actfm);
  k6_down<<<256, 512, 163840, stream>>>(actfm, wd, x1fm, modf, (bf16_t*)d_out);
}
```

```cpp
#include <hip/hip_runtime.h>
#include <stdint.h>

typedef unsigned short bf16_t;
typedef short bf16x8 __attribute__((ext_vector_type(8)));
typedef short s16x4 __attribute__((ext_vector_type(4)));
typedef float f32x2 __attribute__((ext_vector_type(2)));
typedef float f32x4 __attribute__((ext_vector_type(4)));
typedef float f32x16 __attribute__((ext_vector_type(16)));
typedef unsigned u32x2 __attribute__((ext_vector_type(2)));
typedef unsigned u32x4 __attribute__((ext_vector_type(4)));
typedef __bf16 bf16x2_t __attribute__((ext_vector_type(2)));
#define LAS __attribute__((address_space(3)))
typedef LAS const char* lds_cptr;
typedef LAS char* lds_ptr;
#define DI __device__ __forceinline__

constexpr int S_ = 2048, D_ = 1024, F_ = 3584, NMOD = 6144;
constexpr float LN_EPS = 1e-5f;
constexpr float QSCALE = 0.17677669529663687f * 1.4426950408889634f;

constexpr size_t WS_MODP = 0;
constexpr size_t WS_MODF = 128 * 1024;
constexpr size_t WS_CVEC = 160 * 1024;
constexpr size_t WS_WKVF = 192 * 1024;
constexpr size_t WS_KVF  = 384 * 1024;
constexpr size_t WS_UTP  = 640 * 1024;
constexpr size_t WS_STAT = 768 * 1024;
constexpr size_t WS_H    = 1u << 20;
constexpr size_t WS_O    = 5u << 20;
constexpr size_t WS_X1   = 9u << 20;
constexpr size_t WS_Y    = 13u << 20;
constexpr size_t WS_ACT  = 17u << 20;

DI unsigned cvtpk(float lo, float hi) { f32x2 v = {lo, hi}; bf16x2_t b = __builtin_convertvector(v, bf16x2_t); return __builtin_bit_cast(unsigned, b); }
DI float bflo(unsigned u) { return __uint_as_float(u << 16); }
DI float bfhi(unsigned u) { return __uint_as_float(u & 0xffff0000u); }
DI float rbf(float x) { return bflo(cvtpk(x, 0.f) & 0xffffu); }
DI float bf2f(bf16_t b) { return __uint_as_float((unsigned)b << 16); }
DI void glds16(const void* sbase, unsigned voff, unsigned lds_dst) {
  unsigned keep;
  asm volatile("s_mov_b32 %0, m0\n\ts_mov_b32 m0, %3\n\ts_nop 0\n\tglobal_load_lds_dwordx4 %1, %2\n\ts_mov_b32 m0, %0"
               : "=&s"(keep) : "v"(voff), "s"(sbase), "s"(lds_dst) : "memory");
}
#define GLOAD4(dst, sbase, voff, imm) asm volatile("global_load_dwordx4 %0, %1, %2 offset:%3" : "=v"(dst) : "v"(voff), "s"(sbase), "n"(imm) : "memory")
#define GLOAD4NT(dst, sbase, voff) asm volatile("global_load_dwordx4 %0, %1, %2 nt" : "=v"(dst) : "v"(voff), "s"(sbase) : "memory")
#define GLOAD1(dst, sbase, voff) asm volatile("global_load_dword %0, %1, %2" : "=v"(dst) : "v"(voff), "s"(sbase) : "memory")
#define GLOADH(dst, sbase, voff) asm volatile("global_load_ushort %0, %1, %2" : "=v"(dst) : "v"(voff), "s"(sbase) : "memory")
typedef short v4i16_t __attribute__((ext_vector_type(4)));
DI s16x4 ldtr(lds_cptr p) { return __builtin_bit_cast(s16x4, __builtin_amdgcn_ds_read_tr16_b64_v4i16((LAS v4i16_t*)p)); }
DI bf16x8 ldtr2(lds_cptr p0, lds_cptr p1) { s16x4 a = ldtr(p0), b = ldtr(p1); return (bf16x8){a[0], a[1], a[2], a[3], b[0], b[1], b[2], b[3]}; }
DI bf16x8 ld128(lds_cptr p) { return *(const LAS bf16x8*)p; }
#define MFMA32(a, b, c) __builtin_amdgcn_mfma_f32_32x32x16_bf16(a, b, c, 0, 0, 0)
#define MFMA16(a, b, c) __builtin_amdgcn_mfma_f32_16x16x32_bf16(a, b, c, 0, 0, 0)
#define SBAR() __builtin_amdgcn_sched_barrier(0)
#define BAR() asm volatile("s_barrier" ::: "memory")
#define WAITV(n) asm volatile("s_waitcnt vmcnt(" #n ")" ::: "memory")
#define WAITL() asm volatile("s_waitcnt lgkmcnt(0)" ::: "memory")
#define RFL(x) __builtin_amdgcn_readfirstlane(x)
#define PIN(x) asm volatile("" : "+v"(x))
#define PIN2(x, y) asm volatile("" : "+v"(x), "+v"(y))
#define WT 1
DI void store16(void* p, u32x4 v) {
  asm volatile("global_store_dwordx4 %0, %1, off sc1\n\ts_nop 1" :: "v"(p), "v"(v) : "memory");
}
DI float sum4g(float x) {
  auto a = __builtin_amdgcn_permlane16_swap(__float_as_uint(x), __float_as_uint(x), false, false); x = __uint_as_float(a[0]) + __uint_as_float(a[1]);
  auto b = __builtin_amdgcn_permlane32_swap(__float_as_uint(x), __float_as_uint(x), false, false); return __uint_as_float(b[0]) + __uint_as_float(b[1]);
}
template <int CTRL> DI float dpp_add(float x) { return x + __int_as_float(__builtin_amdgcn_update_dpp(0, __float_as_int(x), CTRL, 0xf, 0xf, true)); }
DI float sum64(float x) { x = dpp_add<0xB1>(x); x = dpp_add<0x4E>(x); x = dpp_add<0x141>(x); x = dpp_add<0x140>(x); return sum4g(x); }
#define KEEP_S(p) asm volatile("" :: "s"(p))
#define TOUCH(dummy, sbase, voff) asm volatile("global_load_dword %0, %1, %2" : "+v"(dummy) : "v"(voff), "s"(sbase) : "memory")
#define TOUCH255(sbase, voff) asm volatile("global_load_dword v255, %0, %1" :: "v"(voff), "s"(sbase) : "memory", "v255")
typedef __bf16 bf16x8n __attribute__((ext_vector_type(8)));
typedef __bf16 bf16x16n __attribute__((ext_vector_type(16)));
typedef unsigned u32x8 __attribute__((ext_vector_type(8)));

#define STAMP_ARG
#define STAMP_PASS(k)
#define ST_DECL
#define ST(i)
#define ST_END(i)

#define XTOUCH 1
DI float silu_bf(bf16_t c) { const float cv = bf2f(c); return rbf(cv * __builtin_amdgcn_rcpf(1.f + __expf(-cv))); }
__global__ __launch_bounds__(256, 2) void k1_mod(const bf16_t* __restrict__ c, const bf16_t* __restrict__ wmod, const bf16_t* __restrict__ wk,
                                                 const bf16_t* __restrict__ wv, float* __restrict__ modp, bf16_t* __restrict__ wkvf, const bf16_t* __restrict__ x STAMP_ARG) {
  __shared__ float red[4][64];
  ST_DECL; ST(0);
  KEEP_S(c); KEEP_S(wmod); KEEP_S(wk); KEEP_S(wv); KEEP_S(modp); KEEP_S(wkvf); KEEP_S(x);
  const int b = blockIdx.x, tid = threadIdx.x;
  ST(1);
  if (__builtin_expect(b < 256, 1)) {
    const int cg = b & 63, ks = b >> 6, l8 = tid & 7, r = tid >> 3;
    const bf16_t* wp = wmod + (size_t)(ks * 256 + r) * NMOD + cg * 64 + (cg >= 32 ? 1024 : 0) + l8 * 8;
    u32x4 w[8]; bf16_t cr[8];
#pragma unroll
    for (int i = 0; i < 8; ++i) w[i] = __builtin_nontemporal_load((const u32x4*)(wp + (size_t)i * 32 * NMOD));
#pragma unroll
    for (int i = 0; i < 8; ++i) cr[i] = c[ks * 256 + i * 32 + r];
    SBAR();
    unsigned dummy = 0;
    if (XTOUCH) TOUCH(dummy, x, (unsigned)((b & 7) * 524288 + ((b >> 3) * 128 + (tid >> 6) * 32 + (tid & 31)) * 128));
    ST(3);
    float acc[8] = {0, 0, 0, 0, 0, 0, 0, 0};
#pragma unroll
    for (int i = 0; i < 8; ++i) {
      const float sv = silu_bf(cr[i]);
#pragma unroll
      for (int j = 0; j < 4; ++j) { acc[2 * j] += sv * bflo(w[i][j]); acc[2 * j + 1] += sv * bfhi(w[i][j]); }
    }
#pragma unroll
    for (int j = 0; j < 8; ++j) acc[j] = sum4g(dpp_add<0x128>(acc[j]));
    if ((tid & 63) < 8) { *(f32x4*)&red[tid >> 6][l8 * 8] = (f32x4){acc[0], acc[1], acc[2], acc[3]}; *(f32x4*)&red[tid >> 6][l8 * 8 + 4] = (f32x4){acc[4], acc[5], acc[6], acc[7]}; }
    __syncthreads();
    ST(6);
    if (tid < 64) modp[ks * 4096 + cg * 64 + tid] = (red[0][tid] + red[1][tid]) + (red[2][tid] + red[3][tid]);
    asm volatile("s_waitcnt vmcnt(0)" : "+v"(dummy));
  } else {
    const int it = (b - 256) * 256 + tid, lane = it & 63, ct = (it >> 6) & 1, J = (it >> 7) & 31, z = it >> 12;
    const bf16_t* src = (z ? wv : wk) + (size_t)(32 * J + 8 * (lane >> 4)) * 32 + ct * 16 + (lane & 15);
    unsigned o[4];
#pragma unroll
    for (int j = 0; j < 4; ++j) o[j] = (unsigned)src[(2 * j) * 32] | ((unsigned)src[(2 * j + 1) * 32] << 16);
    *(u32x4*)(wkvf + (size_t)it * 8) = (u32x4){o[0], o[1], o[2], o[3]};
  }
  ST_END(7);
}

__global__ __launch_bounds__(512, 2) void k2_ln_kv(const bf16_t* __restrict__ x, float* __restrict__ modp, const bf16_t* __restrict__ wkvf,
                                                   const bf16_t* __restrict__ wq, bf16_t* __restrict__ hfm, bf16_t* __restrict__ kvf,
                                                   float* __restrict__ modf, bf16_t* __restrict__ cvec, const bf16_t* __restrict__ c, const bf16_t* __restrict__ wmod STAMP_ARG) {
  __shared__ __attribute__((aligned(16))) char smem[34816 + 16384 + 8192 + 2048];
  ST_DECL; ST(0);
  KEEP_S(x); KEEP_S(modp); KEEP_S(wkvf); KEEP_S(wq); KEEP_S(hfm); KEEP_S(kvf); KEEP_S(modf); KEEP_S(cvec); KEEP_S(c); KEEP_S(wmod);
  ST(7);
  const int tid = threadIdx.x, lane = tid & 63, w = RFL(tid >> 6);
  const int xcd = blockIdx.x & 7, bi = blockIdx.x >> 3, z = bi & 1, tt = xcd * 16 + (bi >> 1);
  u32x4 xv[2][2];
#pragma unroll
  for (int rr = 0; rr < 2; ++rr)
#pragma unroll
    for (int hh = 0; hh < 2; ++hh) xv[rr][hh] = *(const u32x4*)(x + (size_t)(tt * 16 + 2 * w + rr) * D_ + hh * 512 + lane * 8);
  f32x4 mp[4];
#pragma unroll
  for (int p = 0; p < 4; ++p) mp[p] = *(const f32x4*)(modp + p * 4096 + tid * 4);
  SBAR();
  float* mt = (float*)(smem + 34816 + 16384);
  float v[2][16], rstd[2];
#pragma unroll
  for (int rr = 0; rr < 2; ++rr)
#pragma unroll
    for (int hh = 0; hh < 2; ++hh)
#pragma unroll
      for (int j = 0; j < 4; ++j) { v[rr][hh * 8 + 2 * j] = bflo(xv[rr][hh][j]); v[rr][hh * 8 + 2 * j + 1] = bfhi(xv[rr][hh][j]); }
  u32x4 wf[8];
  const char* wfp = (const char*)wkvf + (z * 64 + w * 8) * 1024; const unsigned vlin = (unsigned)(lane * 16);
  { f32x4 a = (mp[0] + mp[1]) + (mp[2] + mp[3]);
#pragma unroll
    for (int j = 0; j < 4; ++j) { a[j] = rbf(a[j]); if (w >= 4) a[j] = rbf(1.f + a[j]); }
    *(f32x4*)(mt + tid * 4) = a; }
  { float s0 = 0.f, s1 = 0.f;
#pragma unroll
    for (int j = 0; j < 16; ++j) { s0 += v[0][j]; s1 += v[1][j]; }
    PIN2(s0, s1); SBAR();
#pragma unroll
    for (int i = 0; i < 4; ++i) GLOAD4(wf[i], wfp, vlin, i * 1024);
    SBAR();
    s0 = sum64(s0) * (1.f / D_); s1 = sum64(s1) * (1.f / D_);
    float q0 = 0.f, q1 = 0.f;
#pragma unroll
    for (int j = 0; j < 16; ++j) { v[0][j] -= s0; q0 += v[0][j] * v[0][j]; v[1][j] -= s1; q1 += v[1][j] * v[1][j]; }
    rstd[0] = __builtin_amdgcn_rsqf(sum64(q0) * (1.f / D_) + LN_EPS); rstd[1] = __builtin_amdgcn_rsqf(sum64(q1) * (1.f / D_) + LN_EPS); }
  PIN2(rstd[0], rstd[1]);
  asm volatile("s_waitcnt lgkmcnt(0)\n\ts_barrier" ::: "memory");
  ST(1);
#pragma unroll
  for (int i = 4; i < 8; ++i) GLOAD4(wf[i], wfp + 4096, vlin, (i - 4) * 1024);
  const int gcg = blockIdx.x & 31, gks = blockIdx.x >> 5;
  u32x4 gw[2]; unsigned gc[2], mq[4], dummy;
#pragma unroll
  for (int i = 0; i < 2; ++i) { GLOAD4NT(gw[i], wmod + (size_t)(gks * 128 + i * 64) * NMOD + (gcg < 16 ? 2048 : 4096) + gcg * 64, (unsigned)((tid >> 3) * (NMOD * 2) + (tid & 7) * 16));
    GLOADH(gc[i], c + gks * 128 + i * 64, (unsigned)((tid >> 3) * 2)); }
  const int ce = blockIdx.x * 8 + (lane & 7);
  const int mi = (lane & 8) ? (((ce >> 3) & 1) ? 2048 : 3072) + 32 * (ce >> 6) + 16 * ((ce & 7) >> 2) + 4 * ((ce >> 4) & 3) + (ce & 3) : 3072 + blockIdx.x * 4 + (lane & 3);
#pragma unroll
  for (int p = 0; p < 4; ++p) GLOAD1(mq[p], modp + p * 4096, (unsigned)(mi * 4));
  { const int ln = bi * 256 + w * 32 + (lane & 31); GLOAD1(dummy, wq, (unsigned)((ln >> 3) * 2048 + (xcd & 1) * 1024 + (ln & 7) * 128)); }
  SBAR();
#pragma unroll
  for (int hh = 0; hh < 2; ++hh) {
    const f32x4 s0 = *(const f32x4*)(mt + hh * 512 + lane * 8), s1 = *(const f32x4*)(mt + hh * 512 + lane * 8 + 4);
    const f32x4 c0 = *(const f32x4*)(mt + 1024 + hh * 512 + lane * 8), c1 = *(const f32x4*)(mt + 1024 + hh * 512 + lane * 8 + 4);
#pragma unroll
    for (int rr = 0; rr < 2; ++rr) {
      const float* p = &v[rr][hh * 8]; const float r = rstd[rr];
      const u32x4 o = {cvtpk(p[0] * r * c0[0] + s0[0], p[1] * r * c0[1] + s0[1]), cvtpk(p[2] * r * c0[2] + s0[2], p[3] * r * c0[3] + s0[3]),
                       cvtpk(p[4] * r * c1[0] + s1[0], p[5] * r * c1[1] + s1[1]), cvtpk(p[6] * r * c1[2] + s1[2], p[7] * r * c1[3] + s1[3])};
      *(u32x4*)(smem + (hh * 64 + lane) * 272 + (2 * w + rr) * 16) = o;
    }
  }
  asm volatile("s_waitcnt vmcnt(9) lgkmcnt(0)\n\ts_barrier" : "+v"(wf[0]), "+v"(wf[1]), "+v"(wf[2]), "+v"(wf[3]), "+v"(wf[4]), "+v"(wf[5]), "+v"(wf[6]), "+v"(wf[7]) :: "memory");
  ST(2);
  f32x4 acc[2] = {};
#pragma unroll
  for (int jj = 0; jj < 4; ++jj) {
    const bf16x8 a = *(const bf16x8*)(smem + ((w * 4 + jj) * 4 + (lane >> 4)) * 272 + (lane & 15) * 16);
    acc[0] = MFMA16(a, __builtin_bit_cast(bf16x8, wf[jj * 2]), acc[0]); acc[1] = MFMA16(a, __builtin_bit_cast(bf16x8, wf[jj * 2 + 1]), acc[1]);
  }
  float* red = (float*)(smem + 34816);
  *(f32x4*)(red + ((w * 2) * 64 + lane) * 4) = acc[0]; *(f32x4*)(red + ((w * 2 + 1) * 64 + lane) * 4) = acc[1];
#pragma unroll
  for (int i = 0; i < 2; ++i) { const int it = i * 512 + tid, tok = it & 15, ch = z * 64 + (it >> 4);
    store16(hfm + ((size_t)((ch >> 1) * 64 + (tt >> 1)) * 64 + (tt & 1) * 16 + tok + 32 * (ch & 1)) * 8, *(const u32x4*)(smem + ch * 272 + tok * 16)); }
  asm volatile("s_waitcnt lgkmcnt(0)\n\ts_barrier" ::: "memory");
  ST(3);
  {
    float s = 0.f;
#pragma unroll
    for (int ww = 0; ww < 8; ++ww) s += red[ww * 512 + tid];
    const int r = tid & 3, ln = (tid >> 2) & 63, ct = tid >> 8;
    const int key = tt * 16 + 4 * (ln >> 4) + r, d = ct * 16 + (ln & 15), kt = key >> 5, kl = key & 31;
    int idx;
    if (z == 0) { const int dd = d & 15; idx = ((d >> 4) * 64 + kl + 32 * ((dd >> 2) & 1)) * 8 + (((dd >> 3) << 2) | (dd & 3)); }
    else { const int kk = kl & 15, rho = (d & 3) | (((d >> 3) & 1) << 2) | (((d >> 4) & 1) << 3) | (((d >> 2) & 1) << 4);
           idx = ((2 + (kl >> 4)) * 64 + rho + 32 * ((kk >> 2) & 1)) * 8 + (((kk >> 3) << 2) | (kk & 3)); }
    kvf[(size_t)kt * 2048 + idx] = (bf16_t)(cvtpk(s, 0.f) & 0xffffu);
    asm volatile("s_waitcnt vmcnt(4)" : "+v"(gw[0]), "+v"(gw[1]), "+v"(gc[0]), "+v"(gc[1]), "+v"(mq[0]), "+v"(mq[1]), "+v"(mq[2]), "+v"(mq[3]) :: "memory");
    float ga[8] = {0, 0, 0, 0, 0, 0, 0, 0};
#pragma unroll
    for (int i = 0; i < 2; ++i) { const float sv = silu_bf((bf16_t)gc[i]);
#pragma unroll
      for (int j = 0; j < 4; ++j) { ga[2 * j] += sv * bflo(gw[i][j]); ga[2 * j + 1] += sv * bfhi(gw[i][j]); } }
    float mv = rbf((__uint_as_float(mq[0]) + __uint_as_float(mq[1])) + (__uint_as_float(mq[2]) + __uint_as_float(mq[3]))); if (mi >= 3072) mv = rbf(1.f + mv);
    if (w == 7) { if (lane < 4) modf[1024 + mi] = mv; else if (lane >= 8 && lane < 16) cvec[ce] = (bf16_t)(__float_as_uint(mv) >> 16); }
#pragma unroll
    for (int j = 0; j < 8; ++j) ga[j] = sum4g(dpp_add<0x128>(ga[j]));
    float* gr = (float*)(smem + 34816 + 16384 + 8192);
    if (lane < 8) { *(f32x4*)(gr + w * 64 + lane * 8) = (f32x4){ga[0], ga[1], ga[2], ga[3]}; *(f32x4*)(gr + w * 64 + lane * 8 + 4) = (f32x4){ga[4], ga[5], ga[6], ga[7]}; }
    asm volatile("s_waitcnt lgkmcnt(0)\n\ts_barrier" ::: "memory");
    if (tid < 64) { float t = 0.f;
#pragma unroll
      for (int ww = 0; ww < 8; ++ww) t += gr[ww * 64 + tid];
      modp[16384 + gks * 2048 + gcg * 64 + tid] = t; }
  }
  ST(6);
  asm volatile("s_waitcnt vmcnt(0)" : "+v"(dummy));
  ST_END(4);
}

DI unsigned wimg_src_chunk(int lane) { return (unsigned)(((lane & 7) ^ (((lane >> 4) & 3) << 1)) << 4); }
DI int wimg_rd(int lane, int t) { const int g = lane >> 4, q = (lane & 15) >> 2, r8 = 4 * (g & 1) + q; return (g >> 1) * 1024 + r8 * 128 + ((t ^ (r8 >> 1)) * 32) + (lane & 3) * 8; }

DI bf16x8 pack8(const f32x16& p, int s) {
  u32x4 r = {cvtpk(p[8 * s], p[8 * s + 1]), cvtpk(p[8 * s + 2], p[8 * s + 3]), cvtpk(p[8 * s + 4], p[8 * s + 5]), cvtpk(p[8 * s + 6], p[8 * s + 7])};
  return __builtin_bit_cast(bf16x8, r);
}
DI float rowmax16(const f32x16& p) {
  float a = fmaxf(fmaxf(p[0], p[1]), p[2]), b = fmaxf(fmaxf(p[3], p[4]), p[5]);
  a = fmaxf(fmaxf(a, p[6]), p[7]); b = fmaxf(fmaxf(b, p[8]), p[9]); a = fmaxf(fmaxf(a, p[10]), p[11]); b = fmaxf(fmaxf(b, p[12]), p[13]);
  a = fmaxf(fmaxf(a, p[14]), p[15]); a = fmaxf(a, b);
  auto rr = __builtin_amdgcn_permlane32_swap(__float_as_uint(a), __float_as_uint(a), false, false);
  return fmaxf(__uint_as_float(rr[0]), __uint_as_float(rr[1]));
}
DI bf16x8 plo(const u32x8& p) { return __builtin_bit_cast(bf16x8, __builtin_shufflevector(p, p, 0, 1, 2, 3)); }
DI bf16x8 phi(const u32x8& p) { return __builtin_bit_cast(bf16x8, __builtin_shufflevector(p, p, 4, 5, 6, 7)); }
#define ROWSUM(sel, p, acc, idx) __builtin_amdgcn_smfmac_f32_16x16x64_bf16(__builtin_bit_cast(bf16x8n, sel), __builtin_bit_cast(bf16x16n, p), acc, idx, 0, 0)

__global__ __launch_bounds__(512, 2) void k3_attn(const bf16_t* hfm, const bf16_t* wq, const bf16_t* kvf, bf16_t* ofm, const bf16_t* wg, const bf16_t* wu,
                                                  const bf16_t* cvec, float* utp, const bf16_t* wo, const bf16_t* wd, const bf16_t* x,
                                                  const float* modp, float* modf STAMP_ARG) {
  extern __shared__ __attribute__((aligned(16))) char lds[];
  ST_DECL; ST(0);
  KEEP_S(hfm); KEEP_S(wq); KEEP_S(kvf); KEEP_S(ofm); KEEP_S(wg); KEEP_S(wu); KEEP_S(cvec); KEEP_S(utp); KEEP_S(wo); KEEP_S(wd); KEEP_S(x); KEEP_S(modp); KEEP_S(modf);
  ST(7);
  const int tid = threadIdx.x, lane = tid & 63, w = RFL(tid >> 6), wm = w & 3, wk = w >> 2;
  const int xcd = blockIdx.x & 7, bi = blockIdx.x >> 3, tm = (xcd >> 1) * 4 + (bi & 3), hp = (xcd & 1) * 8 + (bi >> 2);
  const unsigned lds0 = (unsigned)(uintptr_t)lds;
  const lds_cptr L = (lds_cptr)lds;
  const int hi = lane >> 5;
  const unsigned vlin = (unsigned)(lane * 16);
  constexpr int LW = 32768, LUT = 98304;
  const int rot = (bi >> 2) * 4, crot = bi & 7;
  u32x4 hf[32];
  const char* hb = (const char*)hfm + (size_t)(wk * 32 * 64 + tm * 4 + wm) * 1024;
#define HLOAD(i) GLOAD4(hf[i], hb + (size_t)((i) ^ rot) * 65536, vlin, 0)
#define KV_LD(c, slot, q) glds16((const char*)kvf + (((c) + crot) & 7) * 32768 + w * 4096 + (q) * 1024, vlin, lds0 + (slot) * 32768 + w * 4096 + (q) * 1024)
  const unsigned wvoff = (unsigned)((w * 8 + (lane >> 3)) * 2048 + hp * 128 + (((lane & 7) ^ (((lane >> 4) & 1) << 2)) << 4));
#define WLD(q) glds16((const char*)wq + ((q) ^ (rot >> 2)) * 64 * 2048, wvoff, lds0 + LW + ((q) * 8 + w) * 1024)
  WLD(0); WLD(1); WLD(8); WLD(9); HLOAD(0); HLOAD(1); HLOAD(2); HLOAD(3);
  f32x16 qa0 = {}, qa1 = {};
  {
    const int q = (lane & 15) >> 2, pp = lane & 3, nb = (lane >> 4) & 1;
    lds_cptr a0 = L + LW + wk * 65536 + hi * 1024 + q * 128 + (((q >> 1) & 1) * 64) + nb * 32 + pp * 8;
    lds_cptr a1 = L + LW + wk * 65536 + hi * 1024 + q * 128 + ((1 ^ ((q >> 1) & 1)) * 64) + nb * 32 + pp * 8;
    PIN2(a0, a1); PIN2(qa0, qa1);
    asm volatile("s_waitcnt vmcnt(4)\n\ts_barrier" ::: "memory");
    ST(1);
    bf16x8 w0 = ldtr2(a0, a0 + 512), w1 = ldtr2(a1, a1 + 512);
#define QSTEP(i, N, B, LOADS) do { asm volatile("s_waitcnt vmcnt(" #N ")" : "+v"(hf[i])); if (B) BAR(); \
    bf16x8 n0 = w0, n1 = w1; if ((i) < 31) { n0 = ldtr2(a0 + ((i) + 1) * 2048, a0 + ((i) + 1) * 2048 + 512); n1 = ldtr2(a1 + ((i) + 1) * 2048, a1 + ((i) + 1) * 2048 + 512); } SBAR(); \
    qa0 = MFMA32(w0, __builtin_bit_cast(bf16x8, hf[i]), qa0); PIN(qa0); SBAR(); LOADS; qa1 = MFMA32(w1, __builtin_bit_cast(bf16x8, hf[i]), qa1); PIN(qa1); SBAR(); w0 = n0; w1 = n1; } while (0)
    QSTEP(0, 3, 0, WLD(2); HLOAD(4));
    QSTEP(1, 4, 0, WLD(3); HLOAD(5));
    QSTEP(2, 5, 0, WLD(10); HLOAD(6));
    QSTEP(3, 6, 0, WLD(11); HLOAD(7));
    QSTEP(4, 6, 0, WLD(4); HLOAD(8));
    QSTEP(5, 6, 0, HLOAD(9));
    QSTEP(6, 5, 0, WLD(5); HLOAD(10));
    QSTEP(7, 5, 1, HLOAD(11));
    QSTEP(8, 4, 0, WLD(12); HLOAD(12));
    QSTEP(9, 5, 0, HLOAD(13));
    QSTEP(10, 4, 0, WLD(13); HLOAD(14));
    QSTEP(11, 5, 0, HLOAD(15));
    QSTEP(12, 4, 0, WLD(6); HLOAD(16));
    QSTEP(13, 5, 0, HLOAD(17));
    QSTEP(14, 4, 0, WLD(7); HLOAD(18));
    QSTEP(15, 5, 1, HLOAD(19));
    QSTEP(16, 4, 0, WLD(14); HLOAD(20));
    QSTEP(17, 5, 0, HLOAD(21));
    QSTEP(18, 4, 0, WLD(15); HLOAD(22));
    QSTEP(19, 5, 0, HLOAD(23));
    QSTEP(20, 4, 0, KV_LD(0, 0, 0); HLOAD(24));
    QSTEP(21, 5, 0, HLOAD(25));
    QSTEP(22, 4, 0, KV_LD(0, 0, 1); HLOAD(26));
    QSTEP(23, 5, 1, HLOAD(27));
    QSTEP(24, 4, 0, KV_LD(0, 0, 2); HLOAD(28));
    QSTEP(25, 5, 0, HLOAD(29));
    QSTEP(26, 4, 0, KV_LD(0, 0, 3); HLOAD(30));
    QSTEP(27, 5, 0, HLOAD(31));
    QSTEP(28, 4, 0, (void)0);
    QSTEP(29, 3, 0, (void)0);
    QSTEP(30, 1, 0, (void)0);
    QSTEP(31, 0, 0, (void)0);
  }
  asm volatile("s_barrier" ::: "memory");
  ST(2);
  bf16x8 qfa0, qfa1, qfb0, qfb1;
  {
    lds_ptr xb = (lds_ptr)lds + LUT;
#pragma unroll
    for (int i = 0; i < 4; ++i) { *(LAS f32x4*)(xb + w * 8192 + i * 1024 + lane * 16) = (f32x4){qa0[4 * i], qa0[4 * i + 1], qa0[4 * i + 2], qa0[4 * i + 3]};
      *(LAS f32x4*)(xb + w * 8192 + 4096 + i * 1024 + lane * 16) = (f32x4){qa1[4 * i], qa1[4 * i + 1], qa1[4 * i + 2], qa1[4 * i + 3]}; }
    KV_LD(1, 1, 0); KV_LD(1, 1, 1); KV_LD(1, 1, 2); KV_LD(1, 1, 3);
    asm volatile("s_waitcnt vmcnt(4) lgkmcnt(0)\n\ts_barrier" ::: "memory");
#pragma unroll
    for (int i = 0; i < 4; ++i) { const f32x4 o0 = *(const LAS f32x4*)(xb + (w ^ 4) * 8192 + i * 1024 + lane * 16), o1 = *(const LAS f32x4*)(xb + (w ^ 4) * 8192 + 4096 + i * 1024 + lane * 16);
#pragma unroll
      for (int j = 0; j < 4; ++j) { qa0[4 * i + j] = (qa0[4 * i + j] + o0[j]) * QSCALE; qa1[4 * i + j] = (qa1[4 * i + j] + o1[j]) * QSCALE; } }
    qfa0 = pack8(qa0, 0); qfa1 = pack8(qa0, 1); qfb0 = pack8(qa1, 0); qfb1 = pack8(qa1, 1);
  }
  u32x4 sel;
  { const unsigned on = (lane & 2) ? 0u : 0x3f803f80u; const bool od = (lane >> 2) & 1; sel = (u32x4){od ? 0u : on, od ? 0u : on, od ? on : 0u, od ? on : 0u}; }
  const int sidx = (lane & 1) ? 0xEEEE : 0x4444;
  f32x16 oa = {}, ob = {}; f32x4 la = {}, lb = {};
  const lds_cptr kv0 = L + wk * 16384 + lane * 16;
  bf16x8 kf0 = ld128(kv0), kf1 = ld128(kv0 + 1024), vf0 = ld128(kv0 + 2048), vf1 = vf0;
  f32x16 sA, sB, na, nb;
  {
    const f32x16 z = {};
    sA = MFMA32(kf0, qfa0, z); sB = MFMA32(kf0, qfb0, z); sA = MFMA32(kf1, qfa1, sA); sB = MFMA32(kf1, qfb1, sB);
    const float ma = rowmax16(sA), mb = rowmax16(sB);
#pragma unroll
    for (int r = 0; r < 16; ++r) { sA[r] -= ma; na[r] = -ma; nb[r] = -mb; }
    PIN2(na, nb);
  }
  u32x8 pA, pB = {0, 0, 0, 0, 0, 0, 0, 0};
  const int ub = bi < 28 ? bi : bi - 28, ucg = (xcd >> 1) * 14 + (ub % 14);
  const char* usrc = (const char*)(ub < 14 ? wg : wu) + ucg * 128 + (size_t)((xcd & 1) * 512 + wm * 8) * 7168;
  const unsigned uvoff = (unsigned)((lane >> 3) * 7168) + wimg_src_chunk(lane);
  if (wk) TOUCH255((const char*)wd + (blockIdx.x * 4 + wm) * 7168, (unsigned)((lane < 56 ? lane : 55) * 128));
#define SIDE(cw, slot, k) do { if (!wk) { if ((cw) < 6) { const char* s_ = (const char*)kvf + (((cw) + 2 + crot) & 7) * 32768 + wm * 8192 + (k) * 2048; const unsigned d_ = lds0 + (slot) * 32768 + wm * 8192 + (k) * 2048; \
        glds16(s_, vlin, d_); glds16(s_ + 1024, vlin, d_ + 1024); } } \
    else if ((cw) < 4) glds16(usrc + (size_t)((cw) * 4 + (k)) * (32 * 7168), uvoff, lds0 + LUT + (((cw) * 4 + (k)) * 4 + wm) * 1024); \
    else if ((cw) == 5) { if ((k) < 2) TOUCH255((const char*)wo + ((bi * 4 + wm) * 2 + (k)) * 8192, (unsigned)(lane * 128)); \
      else if ((k) == 2) TOUCH255((const char*)x + xcd * 524288 + (bi * 4 + wm) * 4096, (unsigned)((lane & 31) * 128)); } } while (0)
#define EX(S, r) S[r] = __builtin_amdgcn_exp2f(S[r])
#define CV(P, S, r) P[(r) >> 1] = cvtpk(S[r], S[(r) + 1])
#define HS(SC, SN, PP, PC, q0, q1, nm, oac, lac, L0, L1, L2, L3, G) do { \
    SN = MFMA32(kf0, q0, nm); SBAR(); L0; EX(SC, 0); EX(SC, 1); EX(SC, 2); EX(SC, 3); PIN(SC); SBAR(); \
    SN = MFMA32(kf1, q1, SN); SBAR(); L1; EX(SC, 4); EX(SC, 5); CV(PC, SC, 0); CV(PC, SC, 2); EX(SC, 6); PIN2(SC, PC); SBAR(); \
    oac = MFMA32(vf0, plo(PP), oac); SBAR(); L2; EX(SC, 7); EX(SC, 8); CV(PC, SC, 4); EX(SC, 9); CV(PC, SC, 6); PIN2(SC, PC); SBAR(); \
    oac = MFMA32(vf1, phi(PP), oac); SBAR(); L3; EX(SC, 10); EX(SC, 11); CV(PC, SC, 8); EX(SC, 12); CV(PC, SC, 10); PIN2(SC, PC); SBAR(); \
    lac = ROWSUM(sel, PP, lac, sidx); SBAR(); G; EX(SC, 13); EX(SC, 14); EX(SC, 15); CV(PC, SC, 12); CV(PC, SC, 14); PIN(PC); SBAR(); } while (0)
#define STEP(pc, pn, G0, G1) do { HS(sA, sB, pB, pA, qfb0, qfb1, nb, ob, lb, kf0 = ld128(pn), kf1 = ld128((pn) + 1024), vf0 = ld128((pc) + 2048), vf1 = ld128((pc) + 3072), G0); \
    HS(sB, sA, pA, pB, qfa0, qfa1, na, oa, la, (void)0, (void)0, (void)0, (void)0, G1); } while (0)
  ST(3);
  int sl = 0;
#pragma unroll 1
  for (int c = 0; c < 8; ++c) {
    const int s1 = sl == 2 ? 0 : sl + 1, s2 = s1 == 2 ? 0 : s1 + 1;
    const lds_cptr pc = kv0 + sl * 32768, pn = kv0 + s1 * 32768;
    const unsigned cp = (unsigned)(c - 1);
    STEP(pc, pc + 4096, SIDE(cp, s1, 2), SIDE(cp, s1, 3));
    STEP(pc + 4096, pc + 8192, (void)0, (void)0);
    STEP(pc + 8192, pc + 12288, (void)0, (void)0);
    if (c < 7) { if (!wk || c == 0 || c == 5) WAITV(0); BAR(); }
    if (c == 5 && bi < 28 && w < 4) {
      f32x4 ua = {};
      const lds_cptr wr = L + LUT + wimg_rd(lane, w);
      const bf16_t* cr = cvec + (xcd & 1) * 1024 + (lane >> 4) * 16 + (lane & 1) * 8;
      bf16x8 cf[16];
#pragma unroll
      for (int J = 0; J < 16; ++J) cf[J] = *(const bf16x8*)(cr + J * 64);
#pragma unroll
      for (int J = 0; J < 16; ++J) ua = MFMA16(ldtr2(wr + J * 4096, wr + J * 4096 + 2048), cf[J], ua);
      if ((lane & 15) < 2) *(f32x4*)(utp + (((ucg >> 1) * 2 + (xcd & 1)) * 4 + (ub < 14 ? 0 : 2) + (lane & 1)) * 128 + (ucg & 1) * 64 + 16 * w + 4 * (lane >> 4)) = ua;
    }
    if (c == 5 && w == 4) {
      const int gi = blockIdx.x * 8 + (lane & 7);
      const float t = sum4g(dpp_add<0x128>(modp[16384 + (lane >> 3) * 2048 + gi]));
      if (lane < 8) modf[(gi < 1024 ? 2048 : 4096) + gi] = rbf(t);
    }
    SBAR();
    STEP(pc + 12288, pn, SIDE(c, s2, 0), SIDE(c, s2, 1));
    sl = s1;
  }
  ob = MFMA32(vf0, plo(pB), ob); ob = MFMA32(vf1, phi(pB), ob); lb = ROWSUM(sel, pB, lb, sidx);
  ST(4);
  f32x16 oacc; float lsum; bool bad;
  {
    const f32x16 snd = wk ? oa : ob; oacc = wk ? ob : oa;
    const float l_a = la[0] + la[1], l_b = lb[0] + lb[1];
    const float ls = wk ? l_a : l_b, lk = wk ? l_b : l_a, ns = wk ? na[0] : nb[0], nk = wk ? nb[0] : na[0];
    lds_ptr xb = (lds_ptr)lds + LUT;
#pragma unroll
    for (int i = 0; i < 4; ++i) *(LAS f32x4*)(xb + w * 8192 + i * 1024 + lane * 16) = (f32x4){snd[4 * i], snd[4 * i + 1], snd[4 * i + 2], snd[4 * i + 3]};
    *(LAS f32x2*)(xb + w * 8192 + 4096 + lane * 8) = (f32x2){ls, ns};
    asm volatile("s_waitcnt lgkmcnt(0)\n\ts_barrier" ::: "memory");
    const f32x2 pl = *(const LAS f32x2*)(xb + (w ^ 4) * 8192 + 4096 + lane * 8);
    const float nmin = fminf(nk, pl[1]), fk = __builtin_amdgcn_exp2f(nmin - nk), fp = __builtin_amdgcn_exp2f(nmin - pl[1]);
    lsum = lk * fk + pl[0] * fp;
    bad = !(lk < 1.0e12f) || !(pl[0] < 1.0e12f);
#pragma unroll
    for (int i = 0; i < 4; ++i) { const f32x4 o = *(const LAS f32x4*)(xb + (w ^ 4) * 8192 + i * 1024 + lane * 16);
#pragma unroll
      for (int j = 0; j < 4; ++j) oacc[4 * i + j] = oacc[4 * i + j] * fk + o[j] * fp; }
  }
#pragma unroll
  for (int r = 0; r < 16; ++r) bad = bad || !(fabsf(oacc[r]) < 3.0e38f);
  if (__any(bad)) {
    const bf16x8 qf0 = wk ? qfb0 : qfa0, qf1 = wk ? qfb1 : qfa1;
    float m = -INFINITY; lsum = 0.f;
#pragma unroll
    for (int r = 0; r < 16; ++r) oacc[r] = 0.f;
    const bf16_t* g = kvf + lane * 8;
    for (int t = 0; t < 64; ++t) {
      const f32x16 z = {};
      f32x16 s = MFMA32(*(const bf16x8*)(g + t * 2048), qf0, z); s = MFMA32(*(const bf16x8*)(g + t * 2048 + 512), qf1, s);
      const float mn = fmaxf(m, rowmax16(s)), al = __builtin_amdgcn_exp2f(m - mn); m = mn;
#pragma unroll
      for (int r = 0; r < 16; ++r) { s[r] = __builtin_amdgcn_exp2f(s[r] - mn); oacc[r] *= al; }
      u32x8 p;
#pragma unroll
      for (int r = 0; r < 16; r += 2) CV(p, s, r);
      f32x4 lt = {}; lt = ROWSUM(sel, p, lt, sidx);
      lsum = lsum * al + (lt[0] + lt[1]);
      oacc = MFMA32(*(const bf16x8*)(g + t * 2048 + 1024), plo(p), oacc); oacc = MFMA32(*(const bf16x8*)(g + t * 2048 + 1536), phi(p), oacc);
    }
  }
  const float rl = __builtin_amdgcn_rcpf(lsum);
#pragma unroll
  for (int r = 0; r < 16; ++r) oacc[r] *= rl;
  {
    const int m16 = tm * 8 + wm * 2 + ((lane >> 4) & 1), J = hp * 2 + wk;
    bf16_t* op = ofm + ((size_t)(J * 128 + m16) * 64 + (lane & 15) + 32 * hi) * 8;
    store16(op, __builtin_bit_cast(u32x4, pack8(oacc, 0))); store16(op + 128, __builtin_bit_cast(u32x4, pack8(oacc, 1)));
  }
  ST(6);
  ST_END(5);
}

#define GLOAD2(dst, sbase, voff, imm) asm volatile("global_load_dwordx2 %0, %1, %2 offset:%3" : "=v"(dst) : "v"(voff), "s"(sbase), "n"(imm) : "memory")
__global__ __launch_bounds__(512, 2) void k4_oproj(const bf16_t* ofm, const bf16_t* wo, const bf16_t* x, const float* modf, bf16_t* x1fm, bf16_t* yfm, f32x2* stat STAMP_ARG) {
  extern __shared__ __attribute__((aligned(16))) char lds[];
  ST_DECL; ST(0);
  KEEP_S(ofm); KEEP_S(wo); KEEP_S(x); KEEP_S(modf); KEEP_S(x1fm); KEEP_S(yfm); KEEP_S(stat);
  ST(7);
  const int tid = threadIdx.x, lane = tid & 63, w = RFL(tid >> 6), wm = w & 3, wk = w >> 2;
  const int xcd = blockIdx.x & 7, bi = blockIdx.x >> 3, tm = xcd * 2 + (bi & 1), tn = bi >> 1;
  const unsigned lds0 = (unsigned)(uintptr_t)lds;
  const lds_cptr L = (lds_cptr)lds;
  const unsigned vlin = (unsigned)(lane * 16);
  u32x4 af[16][2];
  const char* ab = (const char*)ofm + (size_t)(wk * 16 * 128 + tm * 8 + wm * 2) * 1024;
#define AL(i, h) GLOAD4(af[i][h], ab + (size_t)((i) ^ tn) * 131072, vlin, (h) * 1024)
  const unsigned wvoff = (unsigned)(((w ^ ((tn & 1) << 2)) * 8 + (lane >> 3)) * 2048 + tn * 128) + wimg_src_chunk(lane);
#define OWL(q) glds16((const char*)wo + ((q) ^ (tn >> 1)) * 64 * 2048, wvoff, lds0 + ((q) * 8 + w) * 1024)
  AL(0, 0); AL(0, 1); OWL(0); OWL(1); OWL(8); OWL(9); AL(1, 0); AL(1, 1);
  const int g = lane >> 4, tok = tm * 128 + wm * 32 + wk * 16 + (lane & 15);
  const unsigned xvoff = (unsigned)((lane >> 3) * 2048 + (((lane & 7) ^ (lane >> 3)) << 4)), mvoff = (unsigned)(((lane >> 4) & 1) * 8192 + (lane & 15) * 16);
#define OEL(k) do { if ((k) < 2) glds16((const char*)x + (size_t)(tm * 128 + wm * 32 + wk * 16 + (k) * 8) * 2048 + tn * 128, xvoff, lds0 + 131072 + w * 4096 + (k) * 1024); \
    else glds16(modf + 2048 + tn * 64, mvoff, lds0 + 131072 + w * 4096 + 2048); } while (0)
  f32x4 acc[2][4] = {};
  {
    lds_cptr a[4];
#pragma unroll
    for (int t = 0; t < 4; ++t) { a[t] = L + wk * 65536 + wimg_rd(lane, t); PIN(a[t]); PIN2(acc[0][t], acc[1][t]); }
    asm volatile("s_waitcnt vmcnt(2)\n\ts_barrier" ::: "memory");
    ST(1);
    bf16x8 wf[4], wn[4];
#pragma unroll
    for (int t = 0; t < 4; ++t) wf[t] = ldtr2(a[t], a[t] + 2048);
#define OMM(i, t) do { acc[0][t] = MFMA16(wf[t], __builtin_bit_cast(bf16x8, af[i][0]), acc[0][t]); acc[1][t] = MFMA16(wf[t], __builtin_bit_cast(bf16x8, af[i][1]), acc[1][t]); PIN2(acc[0][t], acc[1][t]); SBAR(); } while (0)
#define OSTEP(i, N, B, L0, L1, L2, L3) do { asm volatile("s_waitcnt vmcnt(" #N ")" : "+v"(af[i][0]), "+v"(af[i][1])); if (B) BAR(); \
    _Pragma("unroll") for (int t = 0; t < 4; ++t) wn[t] = (i) < 15 ? ldtr2(a[t] + ((i) + 1) * 4096, a[t] + ((i) + 1) * 4096 + 2048) : wf[t]; SBAR(); \
    OMM(i, 0); L0; OMM(i, 1); L1; OMM(i, 2); L2; OMM(i, 3); L3; _Pragma("unroll") for (int t = 0; t < 4; ++t) wf[t] = wn[t]; } while (0)
    OSTEP(0, 6, 0, OWL(2), OWL(3), AL(2, 0), AL(2, 1));
    OSTEP(1, 4, 0, OWL(10), OWL(11), AL(3, 0), AL(3, 1));
    OSTEP(2, 4, 0, OWL(4), AL(4, 0), (void)0, AL(4, 1));
    OSTEP(3, 3, 1, OWL(5), AL(5, 0), (void)0, AL(5, 1));
    OSTEP(4, 3, 0, OWL(12), AL(6, 0), (void)0, AL(6, 1));
    OSTEP(5, 3, 0, OWL(13), AL(7, 0), (void)0, AL(7, 1));
    OSTEP(6, 3, 0, OWL(6), AL(8, 0), (void)0, AL(8, 1));
    OSTEP(7, 3, 1, OWL(7), AL(9, 0), (void)0, AL(9, 1));
    OSTEP(8, 3, 0, OWL(14), AL(10, 0), (void)0, AL(10, 1));
    OSTEP(9, 3, 0, OWL(15), AL(11, 0), (void)0, AL(11, 1));
    OSTEP(10, 3, 0, OEL(0), AL(12, 0), (void)0, AL(12, 1));
    OSTEP(11, 3, 1, OEL(1), AL(13, 0), (void)0, AL(13, 1));
    OSTEP(12, 3, 0, OEL(2), AL(14, 0), (void)0, AL(14, 1));
    OSTEP(13, 3, 0, AL(15, 0), (void)0, AL(15, 1), (void)0);
    OSTEP(14, 2, 0, (void)0, (void)0, (void)0, (void)0);
    OSTEP(15, 0, 0, (void)0, (void)0, (void)0, (void)0);
  }
  ST(2);
  lds_ptr xb = (lds_ptr)lds + 131072;
  u32x2 xr[4]; f32x4 gav[4], cbv[4];
  asm volatile("" ::: "memory");
#pragma unroll
  for (int t = 0; t < 4; ++t) { const lds_cptr e = xb + w * 4096;
    xr[t] = *(const LAS u32x2*)(e + (lane & 15) * 128 + (((2 * t + (g >> 1)) ^ (lane & 7)) << 4) + (g & 1) * 8);
    gav[t] = *(const LAS f32x4*)(e + 2048 + t * 64 + g * 16); cbv[t] = *(const LAS f32x4*)(e + 2048 + 256 + t * 64 + g * 16); }
  asm volatile("s_waitcnt lgkmcnt(0)" : "+v"(xr[0]), "+v"(xr[1]), "+v"(xr[2]), "+v"(xr[3]), "+v"(gav[0]), "+v"(gav[1]), "+v"(gav[2]), "+v"(gav[3]),
               "+v"(cbv[0]), "+v"(cbv[1]), "+v"(cbv[2]), "+v"(cbv[3]) :: "memory");
#pragma unroll
  for (int t = 0; t < 4; ++t) *(LAS f32x4*)(xb + w * 4096 + t * 1024 + lane * 16) = wk ? acc[0][t] : acc[1][t];
  asm volatile("s_waitcnt lgkmcnt(0)\n\ts_barrier" ::: "memory");
  float v[16]; float s = 0.f;
#pragma unroll
  for (int t = 0; t < 4; ++t) {
    const f32x4 o = *(const LAS f32x4*)(xb + (w ^ 4) * 4096 + t * 1024 + lane * 16) + (wk ? acc[1][t] : acc[0][t]), ga = gav[t];
    v[4 * t] = rbf(bflo(xr[t][0]) + ga[0] * o[0]); v[4 * t + 1] = rbf(bfhi(xr[t][0]) + ga[1] * o[1]);
    v[4 * t + 2] = rbf(bflo(xr[t][1]) + ga[2] * o[2]); v[4 * t + 3] = rbf(bfhi(xr[t][1]) + ga[3] * o[3]);
    s += (v[4 * t] + v[4 * t + 1]) + (v[4 * t + 2] + v[4 * t + 3]);
  }
  const size_t fo = ((size_t)(tn * 2 * 128 + (tok >> 4)) * 64 + lane) * 8;
#pragma unroll
  for (int jj = 0; jj < 2; ++jj) {
    const float* p = v + 8 * jj; const f32x4 c0 = cbv[2 * jj], c1 = cbv[2 * jj + 1];
    store16(yfm + fo + jj * 65536, (u32x4){cvtpk(p[0] * c0[0], p[1] * c0[1]), cvtpk(p[2] * c0[2], p[3] * c0[3]), cvtpk(p[4] * c1[0], p[5] * c1[1]), cvtpk(p[6] * c1[2], p[7] * c1[3])});
    store16(x1fm + fo + jj * 65536, (u32x4){cvtpk(p[0], p[1]), cvtpk(p[2], p[3]), cvtpk(p[4], p[5]), cvtpk(p[6], p[7])});
  }
  const float mw = sum4g(s) * (1.f / 64.f); float q = 0.f;
#pragma unroll
  for (int j = 0; j < 16; ++j) { const float d = v[j] - mw; q += d * d; }
  q = sum4g(q);
  if (g == 0) stat[((tok >> 4) * 16 + tn) * 16 + (lane & 15)] = (f32x2){mw, q};
  ST(6);
  ST_END(3);
}

__global__ __launch_bounds__(512, 2) void k5_gateup(const bf16_t* yfm, const bf16_t* wg, const bf16_t* wu, const f32x2* stat, const float* utp, bf16_t* actfm STAMP_ARG) {
  extern __shared__ __attribute__((aligned(16))) char lds[];
  ST_DECL; ST(0);
  KEEP_S(yfm); KEEP_S(wg); KEEP_S(wu); KEEP_S(stat); KEEP_S(utp); KEEP_S(actfm);
  ST(7);
  const int tid = threadIdx.x, lane = tid & 63, w = RFL(tid >> 6), wm = w & 3, wn = w >> 2;
  const int xcd = blockIdx.x & 7, bi = blockIdx.x >> 3, tm = (xcd & 1) * 4 + (bi & 3), tn = (xcd >> 1) * 7 + (bi >> 2);
  const unsigned lds0 = (unsigned)(uintptr_t)lds;
  const lds_cptr L = (lds_cptr)lds;
  const char* sA = (const char*)yfm + (size_t)(tm * 16 + 2 * w) * 1024;
  const char* sB = (const char*)(w < 4 ? wg : wu) + (size_t)(tn * 128 + ((w >> 1) & 1) * 64) * 2 + (size_t)(16 * (w & 1)) * 7168;
  const unsigned vA = (unsigned)(lane * 16), vB = (unsigned)((lane >> 3) * 7168) + wimg_src_chunk(lane);
#define K5_ISSUE(s, slot) do { const unsigned d_ = lds0 + (slot) * 32768 + w * 2048; \
    glds16(sA + (size_t)(s) * 131072, vA, d_); glds16(sA + (size_t)(s) * 131072 + 1024, vA, d_ + 1024); \
    glds16(sB + (size_t)(s) * 32 * 7168, vB, d_ + 16384); glds16(sB + (size_t)(s) * 32 * 7168 + 8 * 7168, vB, d_ + 16384 + 1024); } while (0)
  { const char* sS = (const char*)stat + tm * 32768 + w * 4096; const unsigned d_ = lds0 + 3 * 32768 + w * 4096;
    glds16(sS, vA, d_); glds16(sS + 1024, vA, d_ + 1024); glds16(sS + 2048, vA, d_ + 2048); glds16(sS + 3072, vA, d_ + 3072); }
  glds16((const char*)utp + tn * 4096 + (w & 3) * 1024, vA, lds0 + 131072 + w * 1024);
  K5_ISSUE(0, 0); K5_ISSUE(1, 1);
  lds_cptr ra[2], rb[2][4];
#pragma unroll
  for (int h = 0; h < 2; ++h) { ra[h] = L + h * 65536 + wm * 4096 + lane * 16; PIN(ra[h]);
#pragma unroll
    for (int t = 0; t < 4; ++t) { rb[h][t] = L + h * 65536 + 16384 + wn * 4096 + wimg_rd(lane, t); PIN(rb[h][t]); } }
  asm volatile("s_waitcnt vmcnt(8)\n\ts_barrier" ::: "memory");
  K5_ISSUE(2, 2);
  f32x4 ag[4][4], au[4][4]; float rs[4];
  {
    float sd[4], nmean[4];
#pragma unroll
    for (int m = 0; m < 4; ++m) {
      const lds_cptr sp = L + 3 * 32768 + (wm * 4 + m) * 2048 + (lane >> 4) * 512 + (lane & 15) * 8;
      f32x2 st[4];
#pragma unroll
      for (int j = 0; j < 4; ++j) st[j] = *(const LAS f32x2*)(sp + j * 128);
      const float mean = sum4g((st[0][0] + st[1][0]) + (st[2][0] + st[3][0])) * (1.f / 16.f); float q = 0.f;
#pragma unroll
      for (int j = 0; j < 4; ++j) { const float d = st[j][0] - mean; q += st[j][1] + 64.f * d * d; }
      const float var = sum4g(q) * (1.f / D_) + LN_EPS;
      rs[m] = __builtin_amdgcn_rsqf(var); sd[m] = var * rs[m]; nmean[m] = -mean;
    }
#pragma unroll
    for (int t = 0; t < 4; ++t) { const LAS float* p = (const LAS float*)(L + 131072) + wn * 64 + t * 16 + 4 * (lane >> 4);
      const f32x4 ug = *(const LAS f32x4*)(p) + *(const LAS f32x4*)(p + 512), tg = *(const LAS f32x4*)(p + 128) + *(const LAS f32x4*)(p + 640);
      const f32x4 uu = *(const LAS f32x4*)(p + 256) + *(const LAS f32x4*)(p + 768), tu = *(const LAS f32x4*)(p + 384) + *(const LAS f32x4*)(p + 896);
#pragma unroll
      for (int m = 0; m < 4; ++m) { ag[m][t] = tg * sd[m] + ug * nmean[m]; au[m][t] = tu * sd[m] + uu * nmean[m]; PIN2(ag[m][t], au[m][t]); } }
  }
  asm volatile("s_waitcnt vmcnt(8) lgkmcnt(0)\n\ts_barrier" ::: "memory");
  ST(1);
  if (wn) BAR();
#define K5_STEP(slot, sn, LAST) do { \
    bf16x8 xa[4], wgf[4], wuf[4]; \
    _Pragma("unroll") for (int t = 0; t < 4; ++t) { const lds_cptr p_ = rb[(slot) >> 1][t] + ((slot) & 1) * 32768; wgf[t] = ldtr2(p_, p_ + 2048); wuf[t] = ldtr2(p_ + 8192, p_ + 8192 + 2048); } \
    _Pragma("unroll") for (int m = 0; m < 4; ++m) xa[m] = ld128(ra[(slot) >> 1] + ((slot) & 1) * 32768 + m * 1024); \
    if ((sn) < 32) { K5_ISSUE(sn, ((slot) + 3) & 3); WAITV(8); } else WAITV(0); \
    asm volatile("s_waitcnt lgkmcnt(0)\n\ts_barrier" ::: "memory"); SBAR(); \
    __builtin_amdgcn_s_setprio(1); \
    _Pragma("unroll") for (int m = 0; m < 4; ++m) _Pragma("unroll") for (int t = 0; t < 4; ++t) { ag[m][t] = MFMA16(wgf[t], xa[m], ag[m][t]); au[m][t] = MFMA16(wuf[t], xa[m], au[m][t]); } \
    __builtin_amdgcn_s_setprio(0); SBAR(); \
    if (!(LAST)) BAR(); } while (0)
#pragma unroll 1
  for (int s = 0; s < 32; s += 4) {
    K5_STEP(0, s + 3, false);
    K5_STEP(1, s + 4, false);
    K5_STEP(2, s + 5, false);
    K5_STEP(3, s + 6, s == 28 && wn);
  }
  ST(2);
#pragma unroll
  for (int m = 0; m < 4; ++m) {
    const float r2 = rs[m] * rs[m], k0 = -2.302208198f * rs[m], k1 = (0.044715f * k0) * r2, iv = __builtin_amdgcn_rcpf(r2);
#pragma unroll
    for (int jj = 0; jj < 2; ++jj) {
      u32x4 o;
#pragma unroll
      for (int e = 0; e < 4; ++e) {
        const f32x4 a4 = ag[m][2 * jj + (e >> 1)], u4 = au[m][2 * jj + (e >> 1)];
        const f32x2 a = {a4[2 * (e & 1)], a4[2 * (e & 1) + 1]}, u = {u4[2 * (e & 1)], u4[2 * (e & 1) + 1]};
        const f32x2 z = a * ((a * a) * k1 + k0);
        const f32x2 ex = {__builtin_amdgcn_exp2f(z[0]), __builtin_amdgcn_exp2f(z[1])};
        const f32x2 dn = ex * iv + iv;
        const f32x2 rc = {__builtin_amdgcn_rcpf(dn[0]), __builtin_amdgcn_rcpf(dn[1])};
        const f32x2 r = (a * u) * rc;
        o[e] = cvtpk(r[0], r[1]);
      }
      store16(actfm + ((size_t)((tn * 4 + wn * 2 + jj) * 128 + tm * 16 + wm * 4 + m) * 64 + lane) * 8, o);
      SBAR();
    }
  }
  ST(6);
  ST_END(3);
}

#define K6_NI 2
#define K6ROT 0
__global__ __launch_bounds__(512, 2) void k6_down(const bf16_t* actfm, const bf16_t* wd, const bf16_t* x1fm, const float* modf, bf16_t* out STAMP_ARG) {
  extern __shared__ __attribute__((aligned(16))) char lds[];
  ST_DECL; ST(0);
  KEEP_S(actfm); KEEP_S(wd); KEEP_S(x1fm); KEEP_S(modf); KEEP_S(out);
  ST(7);
  const int tid = threadIdx.x, lane = tid & 63, w = RFL(tid >> 6), wm = w & 3, wk = w >> 2;
  const int xcd = blockIdx.x & 7, bi = blockIdx.x >> 3, tm = (xcd >> 1) * 4 + (bi & 3), tn = (xcd & 1) * 8 + (bi >> 2);
  const unsigned lds0 = (unsigned)(uintptr_t)lds;
  const lds_cptr L = (lds_cptr)lds;
  const char* sW = (const char*)wd + (size_t)(16 * w) * 2048 + tn * 128;
  const unsigned vW = (unsigned)((lane >> 3) * 2048) + wimg_src_chunk(lane);
  const char* sA = (const char*)actfm + ((size_t)(2 * wk) * 128 + tm * 8 + wm * 2) * 1024;
  const unsigned vA = (unsigned)(lane * 16);
  u32x4 ar[4][4];
  int win = 0, pos = K6ROT == 1 ? 2 * (bi & 3) : 0, sl = K6ROT ? (pos + (bi >> 2) + 2 * (bi & 3)) % 7 : 0;
  const char* w_; const char* a_; unsigned d_;
#define K6_NEXT(slot) do { const int st_ = win + sl; d_ = lds0 + (slot) * 16384 + w * 2048; w_ = sW + (size_t)st_ * 128 * 2048; a_ = sA + (size_t)st_ * 524288; \
    sl = sl == 6 ? 0 : sl + 1; if (pos == 6) { pos = 0; win = win == 21 ? 0 : win + 7; } else ++pos; } while (0)
#define K6_LD(u, k) do { if ((k) == 0) glds16(w_, vW, d_); else if ((k) == 1) glds16(w_ + 8 * 2048, vW, d_ + 1024); \
    else if ((k) == 2) GLOAD4(ar[u][0], a_, vA, 0); else if ((k) == 3) GLOAD4(ar[u][1], a_, vA, 1024); \
    else if ((k) == 4) GLOAD4(ar[u][2], a_ + 131072, vA, 0); else GLOAD4(ar[u][3], a_ + 131072, vA, 1024); } while (0)
#define K6_ISSUE(u, slot) do { K6_NEXT(slot); K6_LD(u, 0); K6_LD(u, 1); K6_LD(u, 2); K6_LD(u, 3); K6_LD(u, 4); K6_LD(u, 5); } while (0)
  K6_ISSUE(0, 0); K6_ISSUE(1, 1); K6_ISSUE(2, 2);
  f32x4 acc[2][4] = {};
  lds_cptr a[4];
#pragma unroll
  for (int t = 0; t < 4; ++t) a[t] = L + wk * 8192 + wimg_rd(lane, t);
  const int g = lane >> 4, tok = tm * 128 + wm * 32 + wk * 16 + (lane & 15), col0 = tn * 64 + 4 * g;
  u32x4 xr[2], gbv[4];
  const char* x_ = (const char*)x1fm + (size_t)(tn * 2 * 128 + tm * 8 + wm * 2 + wk) * 1024; const unsigned mo_ = (unsigned)(col0 * 4);
#define K6_EL(k) do { if ((k) == 0) GLOAD4(xr[0], x_, vA, 0); else if ((k) == 1) GLOAD4(xr[1], x_ + 131072, vA, 0); else GLOAD4(gbv[(k) - 2], modf + 5120, mo_, 64 * ((k) - 2)); } while (0)
#define K6_MM(u, i, t) do { acc[0][t] = MFMA16(wf[i][t], __builtin_bit_cast(bf16x8, ar[u][2 * (i)]), acc[0][t]); acc[1][t] = MFMA16(wf[i][t], __builtin_bit_cast(bf16x8, ar[u][2 * (i) + 1]), acc[1][t]); \
    PIN2(acc[0][t], acc[1][t]); SBAR(); } while (0)
#define K6_L(u, MODE, k) do { if ((MODE) == 1) K6_LD(((u) + 3) & 3, k); else if ((MODE) == 2) K6_EL(k); SBAR(); } while (0)
#define K6_STEP(u, WN, MODE) do { \
    asm volatile("s_waitcnt vmcnt(" #WN ")\n\ts_barrier" : "+v"(ar[u][0]), "+v"(ar[u][1]), "+v"(ar[u][2]), "+v"(ar[u][3]) :: "memory"); \
    bf16x8 wf[2][4]; \
    _Pragma("unroll") for (int i = 0; i < 2; ++i) _Pragma("unroll") for (int t = 0; t < 4; ++t) wf[i][t] = ldtr2(a[t] + (u) * 16384 + i * 4096, a[t] + (u) * 16384 + i * 4096 + 2048); \
    if ((MODE) == 1) K6_NEXT(((u) + 3) & 3); SBAR(); \
    K6_L(u, MODE, 0); K6_MM(u, 0, 0); K6_L(u, MODE, 1); K6_MM(u, 0, 1); K6_MM(u, 0, 2); K6_L(u, MODE, 2); K6_MM(u, 0, 3); K6_L(u, MODE, 3); \
    K6_MM(u, 1, 0); K6_MM(u, 1, 1); K6_L(u, MODE, 4); K6_MM(u, 1, 2); K6_L(u, MODE, 5); K6_MM(u, 1, 3); } while (0)
  for (int s = 0; s < 24; s += 4) { K6_STEP(0, 12, 1); K6_STEP(1, 12, 1); K6_STEP(2, 12, 1); K6_STEP(3, 12, 1); }
  K6_STEP(0, 12, 1); K6_STEP(1, 12, 2); K6_STEP(2, 12, 0); K6_STEP(3, 6, 0);
  ST(2);
  lds_ptr xb = (lds_ptr)lds + 131072;
#pragma unroll
  for (int t = 0; t < 4; ++t) *(LAS f32x4*)(xb + w * 4096 + t * 1024 + lane * 16) = wk ? acc[0][t] : acc[1][t];
  asm volatile("s_waitcnt vmcnt(0) lgkmcnt(0)\n\ts_barrier" : "+v"(xr[0]), "+v"(xr[1]), "+v"(gbv[0]), "+v"(gbv[1]), "+v"(gbv[2]), "+v"(gbv[3]) :: "memory");
#pragma unroll
  for (int jj = 0; jj < 2; ++jj) {
    unsigned pk[2][2];
#pragma unroll
    for (int tt = 0; tt < 2; ++tt) { const int t = 2 * jj + tt;
      const f32x4 o = *(const LAS f32x4*)(xb + (w ^ 4) * 4096 + t * 1024 + lane * 16) + (wk ? acc[1][t] : acc[0][t]), gb = __builtin_bit_cast(f32x4, gbv[t]);
      pk[tt][0] = cvtpk(bflo(xr[jj][2 * tt]) + gb[0] * o[0], bfhi(xr[jj][2 * tt]) + gb[1] * o[1]);
      pk[tt][1] = cvtpk(bflo(xr[jj][2 * tt + 1]) + gb[2] * o[2], bfhi(xr[jj][2 * tt + 1]) + gb[3] * o[3]); }
    auto r0 = __builtin_amdgcn_permlane16_swap(pk[0][0], pk[1][0], false, false);
    auto r1 = __builtin_amdgcn_permlane16_swap(pk[0][1], pk[1][1], false, false);
    *(u32x4*)(out + (size_t)tok * D_ + tn * 64 + jj * 32 + (g & 1) * 16 + (g >> 1) * 8) = (u32x4){r0[0], r1[0], r0[1], r1[1]};
  }
  ST(6);
  ST_END(3);
}

extern "C" void kernel_launch(void* const* d_in, const int* in_sizes, int n_in, void* d_out, int out_size, void* d_ws, size_t ws_size, hipStream_t stream) {
  const bf16_t* x = (const bf16_t*)d_in[0]; const bf16_t* wq = (const bf16_t*)d_in[1]; const bf16_t* wk = (const bf16_t*)d_in[2];
  const bf16_t* wv = (const bf16_t*)d_in[3]; const bf16_t* wo = (const bf16_t*)d_in[4]; const bf16_t* wg = (const bf16_t*)d_in[6];
  const bf16_t* wu = (const bf16_t*)d_in[7]; const bf16_t* wd = (const bf16_t*)d_in[8]; const bf16_t* c = (const bf16_t*)d_in[9];
  const bf16_t* wmod = (const bf16_t*)d_in[10];
  char* ws = (char*)d_ws;
  float* modp = (float*)(ws + WS_MODP); float* modf = (float*)(ws + WS_MODF); bf16_t* cvec = (bf16_t*)(ws + WS_CVEC);
  bf16_t* wkvf = (bf16_t*)(ws + WS_WKVF); bf16_t* kvf = (bf16_t*)(ws + WS_KVF);
  float* utp = (float*)(ws + WS_UTP); f32x2* stat = (f32x2*)(ws + WS_STAT);
  bf16_t* hfm = (bf16_t*)(ws + WS_H); bf16_t* ofm = (bf16_t*)(ws + WS_O); bf16_t* x1fm = (bf16_t*)(ws + WS_X1); bf16_t* yfm = (bf16_t*)(ws + WS_Y); bf16_t* actfm = (bf16_t*)(ws + WS_ACT);
  (void)hipFuncSetAttribute((const void*)k3_attn, hipFuncAttributeMaxDynamicSharedMemorySize, 163840);
  (void)hipFuncSetAttribute((const void*)k4_oproj, hipFuncAttributeMaxDynamicSharedMemorySize, 163840);
  (void)hipFuncSetAttribute((const void*)k5_gateup, hipFuncAttributeMaxDynamicSharedMemorySize, 139264);
  (void)hipFuncSetAttribute((const void*)k6_down, hipFuncAttributeMaxDynamicSharedMemorySize, 163840);
  k1_mod<<<288, 256, 0, stream>>>(c, wmod, wk, wv, modp, wkvf, x STAMP_PASS(0));
  k2_ln_kv<<<256, 512, 0, stream>>>(x, modp, wkvf, wq, hfm, kvf, modf, cvec, c, wmod STAMP_PASS(1));
  k3_attn<<<256, 512, 163840, stream>>>(hfm, wq, kvf, ofm, wg, wu, cvec, utp, wo, wd, x, modp, modf STAMP_PASS(2));
  k4_oproj<<<256, 512, 163840, stream>>>(ofm, wo, x, modf, x1fm, yfm, stat STAMP_PASS(3));
  k5_gateup<<<224, 512, 139264, stream>>>(yfm, wg, wu, stat, utp, actfm STAMP_PASS(4));
  k6_down<<<256, 512, 163840, stream>>>(actfm, wd, x1fm, modf, (bf16_t*)d_out STAMP_PASS(5));
}
```

```cpp
#include <hip/hip_runtime.h>
#include <stdint.h>

typedef unsigned short bf16_t;
typedef short bf16x8 __attribute__((ext_vector_type(8)));
typedef short s16x4 __attribute__((ext_vector_type(4)));
typedef float f32x2 __attribute__((ext_vector_type(2)));
typedef float f32x4 __attribute__((ext_vector_type(4)));
typedef float f32x16 __attribute__((ext_vector_type(16)));
typedef unsigned u32x2 __attribute__((ext_vector_type(2)));
typedef unsigned u32x4 __attribute__((ext_vector_type(4)));
typedef __bf16 bf16x2_t __attribute__((ext_vector_type(2)));
#define LAS __attribute__((address_space(3)))
typedef LAS const char* lds_cptr;
typedef LAS char* lds_ptr;
#define DI __device__ __forceinline__

constexpr int S_ = 2048, D_ = 1024, F_ = 3584, NMOD = 6144;
constexpr float LN_EPS = 1e-5f;
constexpr float QSCALE = 0.17677669529663687f * 1.4426950408889634f;

constexpr size_t WS_MODP = 0;
constexpr size_t WS_MODF = 128 * 1024;
constexpr size_t WS_CVEC = 160 * 1024;
constexpr size_t WS_WKVF = 192 * 1024;
constexpr size_t WS_KVF  = 384 * 1024;
constexpr size_t WS_UTF  = 640 * 1024;
constexpr size_t WS_UTP  = 32u << 20;
constexpr size_t WS_FLAG = 33u << 20;
#define PARTOFF 1
constexpr size_t WS_PART = (size_t)PARTOFF << 20;
constexpr size_t WS_STAT = 768 * 1024;
constexpr size_t WS_H    = 1u << 20;
constexpr size_t WS_O    = 5u << 20;
constexpr size_t WS_X1   = 9u << 20;
constexpr size_t WS_Y    = 13u << 20;
constexpr size_t WS_ACT  = 17u << 20;

DI unsigned cvtpk(float lo, float hi) { f32x2 v = {lo, hi}; bf16x2_t b = __builtin_convertvector(v, bf16x2_t); return __builtin_bit_cast(unsigned, b); }
DI float bflo(unsigned u) { return __uint_as_float(u << 16); }
DI float bfhi(unsigned u) { return __uint_as_float(u & 0xffff0000u); }
DI float rbf(float x) { return bflo(cvtpk(x, 0.f) & 0xffffu); }
DI float bf2f(bf16_t b) { return __uint_as_float((unsigned)b << 16); }
DI void glds16(const void* sbase, unsigned voff, unsigned lds_dst) {
  unsigned keep;
  asm volatile("s_mov_b32 %0, m0\n\ts_mov_b32 m0, %3\n\ts_nop 0\n\tglobal_load_lds_dwordx4 %1, %2\n\ts_mov_b32 m0, %0"
               : "=&s"(keep) : "v"(voff), "s"(sbase), "s"(lds_dst) : "memory");
}
#define GLOAD4(dst, sbase, voff, imm) asm volatile("global_load_dwordx4 %0, %1, %2 offset:%3" : "=v"(dst) : "v"(voff), "s"(sbase), "n"(imm) : "memory")
#define GLOAD4NT(dst, sbase, voff) asm volatile("global_load_dwordx4 %0, %1, %2 nt" : "=v"(dst) : "v"(voff), "s"(sbase) : "memory")
#define GLOAD1(dst, sbase, voff) asm volatile("global_load_dword %0, %1, %2" : "=v"(dst) : "v"(voff), "s"(sbase) : "memory")
#define GLOADH(dst, sbase, voff) asm volatile("global_load_ushort %0, %1, %2" : "=v"(dst) : "v"(voff), "s"(sbase) : "memory")
typedef short v4i16_t __attribute__((ext_vector_type(4)));
DI s16x4 ldtr(lds_cptr p) { return __builtin_bit_cast(s16x4, __builtin_amdgcn_ds_read_tr16_b64_v4i16((LAS v4i16_t*)p)); }
DI bf16x8 ldtr2(lds_cptr p0, lds_cptr p1) { s16x4 a = ldtr(p0), b = ldtr(p1); return (bf16x8){a[0], a[1], a[2], a[3], b[0], b[1], b[2], b[3]}; }
DI bf16x8 ld128(lds_cptr p) { return *(const LAS bf16x8*)p; }
#define MFMA32(a, b, c) __builtin_amdgcn_mfma_f32_32x32x16_bf16(a, b, c, 0, 0, 0)
#define MFMA16(a, b, c) __builtin_amdgcn_mfma_f32_16x16x32_bf16(a, b, c, 0, 0, 0)
#define SBAR() __builtin_amdgcn_sched_barrier(0)
#define BAR() asm volatile("s_barrier" ::: "memory")
#define WAITV(n) asm volatile("s_waitcnt vmcnt(" #n ")" ::: "memory")
#define WAITL() asm volatile("s_waitcnt lgkmcnt(0)" ::: "memory")
#define RFL(x) __builtin_amdgcn_readfirstlane(x)
#define PIN(x) asm volatile("" : "+v"(x))
#define PIN2(x, y) asm volatile("" : "+v"(x), "+v"(y))
#define WT 1
DI void store16(void* p, u32x4 v) {
  asm volatile("global_store_dwordx4 %0, %1, off sc1\n\ts_nop 1" :: "v"(p), "v"(v) : "memory");
}
DI float sum4g(float x) {
  auto a = __builtin_amdgcn_permlane16_swap(__float_as_uint(x), __float_as_uint(x), false, false); x = __uint_as_float(a[0]) + __uint_as_float(a[1]);
  auto b = __builtin_amdgcn_permlane32_swap(__float_as_uint(x), __float_as_uint(x), false, false); return __uint_as_float(b[0]) + __uint_as_float(b[1]);
}
template <int CTRL> DI float dpp_add(float x) { return x + __int_as_float(__builtin_amdgcn_update_dpp(0, __float_as_int(x), CTRL, 0xf, 0xf, true)); }
DI float sum64(float x) { x = dpp_add<0xB1>(x); x = dpp_add<0x4E>(x); x = dpp_add<0x141>(x); x = dpp_add<0x140>(x); return sum4g(x); }
#define KEEP_S(p) asm volatile("" :: "s"(p))
#define TOUCH(dummy, sbase, voff) asm volatile("global_load_dword %0, %1, %2" : "+v"(dummy) : "v"(voff), "s"(sbase) : "memory")
#define TOUCH255(sbase, voff) asm volatile("global_load_dword v255, %0, %1" :: "v"(voff), "s"(sbase) : "memory", "v255")
typedef __bf16 bf16x8n __attribute__((ext_vector_type(8)));
typedef __bf16 bf16x16n __attribute__((ext_vector_type(16)));
typedef unsigned u32x8 __attribute__((ext_vector_type(8)));

#define STAMP_ARG
#define STAMP_PASS(k)
#define ST_DECL
#define ST(i)
#define ST_END(i)

#define XTOUCH 1
DI float silu_bf(bf16_t c) { const float cv = bf2f(c); return rbf(cv * __builtin_amdgcn_rcpf(1.f + __expf(-cv))); }
__global__ __launch_bounds__(256, 2) void k1_mod(const bf16_t* __restrict__ c, const bf16_t* __restrict__ wmod, const bf16_t* __restrict__ wk,
                                                 const bf16_t* __restrict__ wv, float* __restrict__ modp, bf16_t* __restrict__ wkvf, const bf16_t* __restrict__ x STAMP_ARG) {
  __shared__ float red[4][64];
  ST_DECL; ST(0);
  KEEP_S(c); KEEP_S(wmod); KEEP_S(wk); KEEP_S(wv); KEEP_S(modp); KEEP_S(wkvf); KEEP_S(x);
  const int b = blockIdx.x, tid = threadIdx.x;
  ST(1);
  if (__builtin_expect(b < 256, 1)) {
    const int cg = b & 63, ks = b >> 6, l8 = tid & 7, r = tid >> 3;
    const bf16_t* wp = wmod + (size_t)(ks * 256 + r) * NMOD + cg * 64 + (cg >= 32 ? 1024 : 0) + l8 * 8;
    u32x4 w[8]; bf16_t cr[8];
#pragma unroll
    for (int i = 0; i < 8; ++i) w[i] = __builtin_nontemporal_load((const u32x4*)(wp + (size_t)i * 32 * NMOD));
#pragma unroll
    for (int i = 0; i < 8; ++i) cr[i] = c[ks * 256 + i * 32 + r];
    SBAR();
    unsigned dummy = 0;
    if (XTOUCH) TOUCH(dummy, x, (unsigned)((b & 7) * 524288 + ((b >> 3) * 128 + (tid >> 6) * 32 + (tid & 31)) * 128));
    ST(3);
    float acc[8] = {0, 0, 0, 0, 0, 0, 0, 0};
#pragma unroll
    for (int i = 0; i < 8; ++i) {
      const float sv = silu_bf(cr[i]);
#pragma unroll
      for (int j = 0; j < 4; ++j) { acc[2 * j] += sv * bflo(w[i][j]); acc[2 * j + 1] += sv * bfhi(w[i][j]); }
    }
#pragma unroll
    for (int j = 0; j < 8; ++j) acc[j] = sum4g(dpp_add<0x128>(acc[j]));
    if ((tid & 63) < 8) { *(f32x4*)&red[tid >> 6][l8 * 8] = (f32x4){acc[0], acc[1], acc[2], acc[3]}; *(f32x4*)&red[tid >> 6][l8 * 8 + 4] = (f32x4){acc[4], acc[5], acc[6], acc[7]}; }
    __syncthreads();
    ST(6);
    if (tid < 64) modp[ks * 4096 + cg * 64 + tid] = (red[0][tid] + red[1][tid]) + (red[2][tid] + red[3][tid]);
    asm volatile("s_waitcnt vmcnt(0)" : "+v"(dummy));
  } else {
    const int it = (b - 256) * 256 + tid, lane = it & 63, ct = (it >> 6) & 1, J = (it >> 7) & 31, z = it >> 12;
    const bf16_t* src = (z ? wv : wk) + (size_t)(32 * J + 8 * (lane >> 4)) * 32 + ct * 16 + (lane & 15);
    unsigned o[4];
#pragma unroll
    for (int j = 0; j < 4; ++j) o[j] = (unsigned)src[(2 * j) * 32] | ((unsigned)src[(2 * j + 1) * 32] << 16);
    *(u32x4*)(wkvf + (size_t)it * 8) = (u32x4){o[0], o[1], o[2], o[3]};
  }
  ST_END(7);
}

__global__ __launch_bounds__(512, 2) void k2_ln_kv(const bf16_t* __restrict__ x, float* __restrict__ modp, const bf16_t* __restrict__ wkvf,
                                                   const bf16_t* __restrict__ wq, bf16_t* __restrict__ hfm, bf16_t* __restrict__ kvf,
                                                   float* __restrict__ modf, bf16_t* __restrict__ cvec, const bf16_t* __restrict__ c, const bf16_t* __restrict__ wmod STAMP_ARG) {
  __shared__ __attribute__((aligned(16))) char smem[34816 + 16384 + 8192 + 2048];
  ST_DECL; ST(0);
  KEEP_S(x); KEEP_S(modp); KEEP_S(wkvf); KEEP_S(wq); KEEP_S(hfm); KEEP_S(kvf); KEEP_S(modf); KEEP_S(cvec); KEEP_S(c); KEEP_S(wmod);
  ST(7);
  const int tid = threadIdx.x, lane = tid & 63, w = RFL(tid >> 6);
  const int xcd = blockIdx.x & 7, bi = blockIdx.x >> 3, z = bi & 1, tt = xcd * 16 + (bi >> 1);
  u32x4 xv[2][2];
#pragma unroll
  for (int rr = 0; rr < 2; ++rr)
#pragma unroll
    for (int hh = 0; hh < 2; ++hh) xv[rr][hh] = *(const u32x4*)(x + (size_t)(tt * 16 + 2 * w + rr) * D_ + hh * 512 + lane * 8);
  f32x4 mp[4];
#pragma unroll
  for (int p = 0; p < 4; ++p) mp[p] = *(const f32x4*)(modp + p * 4096 + tid * 4);
  SBAR();
  float* mt = (float*)(smem + 34816 + 16384);
  float v[2][16], rstd[2];
#pragma unroll
  for (int rr = 0; rr < 2; ++rr)
#pragma unroll
    for (int hh = 0; hh < 2; ++hh)
#pragma unroll
      for (int j = 0; j < 4; ++j) { v[rr][hh * 8 + 2 * j] = bflo(xv[rr][hh][j]); v[rr][hh * 8 + 2 * j + 1] = bfhi(xv[rr][hh][j]); }
  u32x4 wf[8];
  const char* wfp = (const char*)wkvf + (z * 64 + w * 8) * 1024; const unsigned vlin = (unsigned)(lane * 16);
  { f32x4 a = (mp[0] + mp[1]) + (mp[2] + mp[3]);
#pragma unroll
    for (int j = 0; j < 4; ++j) { a[j] = rbf(a[j]); if (w >= 4) a[j] = rbf(1.f + a[j]); }
    *(f32x4*)(mt + tid * 4) = a; }
  { float s0 = 0.f, s1 = 0.f;
#pragma unroll
    for (int j = 0; j < 16; ++j) { s0 += v[0][j]; s1 += v[1][j]; }
    PIN2(s0, s1); SBAR();
#pragma unroll
    for (int i = 0; i < 4; ++i) GLOAD4(wf[i], wfp, vlin, i * 1024);
    SBAR();
    s0 = sum64(s0) * (1.f / D_); s1 = sum64(s1) * (1.f / D_);
    float q0 = 0.f, q1 = 0.f;
#pragma unroll
    for (int j = 0; j < 16; ++j) { v[0][j] -= s0; q0 += v[0][j] * v[0][j]; v[1][j] -= s1; q1 += v[1][j] * v[1][j]; }
    rstd[0] = __builtin_amdgcn_rsqf(sum64(q0) * (1.f / D_) + LN_EPS); rstd[1] = __builtin_amdgcn_rsqf(sum64(q1) * (1.f / D_) + LN_EPS); }
  PIN2(rstd[0], rstd[1]);
  asm volatile("s_waitcnt lgkmcnt(0)\n\ts_barrier" ::: "memory");
  ST(1);
#pragma unroll
  for (int i = 4; i < 8; ++i) GLOAD4(wf[i], wfp + 4096, vlin, (i - 4) * 1024);
  const int gcg = blockIdx.x & 31, gks = blockIdx.x >> 5;
  u32x4 gw[2]; unsigned gc[2], mq[4], dummy;
#pragma unroll
  for (int i = 0; i < 2; ++i) { GLOAD4NT(gw[i], wmod + (size_t)(gks * 128 + i * 64) * NMOD + (gcg < 16 ? 2048 : 4096) + gcg * 64, (unsigned)((tid >> 3) * (NMOD * 2) + (tid & 7) * 16));
    GLOADH(gc[i], c + gks * 128 + i * 64, (unsigned)((tid >> 3) * 2)); }
  const int ce = blockIdx.x * 8 + (lane & 7);
  const int mi = (lane & 8) ? (((ce >> 3) & 1) ? 2048 : 3072) + 32 * (ce >> 6) + 16 * ((ce & 7) >> 2) + 4 * ((ce >> 4) & 3) + (ce & 3) : 3072 + blockIdx.x * 4 + (lane & 3);
#pragma unroll
  for (int p = 0; p < 4; ++p) GLOAD1(mq[p], modp + p * 4096, (unsigned)(mi * 4));
  { const int ln = bi * 256 + w * 32 + (lane & 31); GLOAD1(dummy, wq, (unsigned)((ln >> 3) * 2048 + (xcd & 1) * 1024 + (ln & 7) * 128)); }
  SBAR();
#pragma unroll
  for (int hh = 0; hh < 2; ++hh) {
    const f32x4 s0 = *(const f32x4*)(mt + hh * 512 + lane * 8), s1 = *(const f32x4*)(mt + hh * 512 + lane * 8 + 4);
    const f32x4 c0 = *(const f32x4*)(mt + 1024 + hh * 512 + lane * 8), c1 = *(const f32x4*)(mt + 1024 + hh * 512 + lane * 8 + 4);
#pragma unroll
    for (int rr = 0; rr < 2; ++rr) {
      const float* p = &v[rr][hh * 8]; const float r = rstd[rr];
      const u32x4 o = {cvtpk(p[0] * r * c0[0] + s0[0], p[1] * r * c0[1] + s0[1]), cvtpk(p[2] * r * c0[2] + s0[2], p[3] * r * c0[3] + s0[3]),
                       cvtpk(p[4] * r * c1[0] + s1[0], p[5] * r * c1[1] + s1[1]), cvtpk(p[6] * r * c1[2] + s1[2], p[7] * r * c1[3] + s1[3])};
      *(u32x4*)(smem + (hh * 64 + lane) * 272 + (2 * w + rr) * 16) = o;
    }
  }
  asm volatile("s_waitcnt vmcnt(9) lgkmcnt(0)\n\ts_barrier" : "+v"(wf[0]), "+v"(wf[1]), "+v"(wf[2]), "+v"(wf[3]), "+v"(wf[4]), "+v"(wf[5]), "+v"(wf[6]), "+v"(wf[7]) :: "memory");
  ST(2);
  f32x4 acc[2] = {};
#pragma unroll
  for (int jj = 0; jj < 4; ++jj) {
    const bf16x8 a = *(const bf16x8*)(smem + ((w * 4 + jj) * 4 + (lane >> 4)) * 272 + (lane & 15) * 16);
    acc[0] = MFMA16(a, __builtin_bit_cast(bf16x8, wf[jj * 2]), acc[0]); acc[1] = MFMA16(a, __builtin_bit_cast(bf16x8, wf[jj * 2 + 1]), acc[1]);
  }
  float* red = (float*)(smem + 34816);
  *(f32x4*)(red + ((w * 2) * 64 + lane) * 4) = acc[0]; *(f32x4*)(red + ((w * 2 + 1) * 64 + lane) * 4) = acc[1];
#pragma unroll
  for (int i = 0; i < 2; ++i) { const int it = i * 512 + tid, tok = it & 15, ch = z * 64 + (it >> 4);
    store16(hfm + ((size_t)((ch >> 1) * 64 + (tt >> 1)) * 64 + (tt & 1) * 16 + tok + 32 * (ch & 1)) * 8, *(const u32x4*)(smem + ch * 272 + tok * 16)); }
  asm volatile("s_waitcnt lgkmcnt(0)\n\ts_barrier" ::: "memory");
  ST(3);
  {
    float s = 0.f;
#pragma unroll
    for (int ww = 0; ww < 8; ++ww) s += red[ww * 512 + tid];
    const int r = tid & 3, ln = (tid >> 2) & 63, ct = tid >> 8;
    const int key = tt * 16 + 4 * (ln >> 4) + r, d = ct * 16 + (ln & 15), kt = key >> 5, kl = key & 31;
    int idx;
    if (z == 0) { const int dd = d & 15; idx = ((d >> 4) * 64 + kl + 32 * ((dd >> 2) & 1)) * 8 + (((dd >> 3) << 2) | (dd & 3)); }
    else { const int kk = kl & 15, rho = (d & 3) | (((d >> 3) & 1) << 2) | (((d >> 4) & 1) << 3) | (((d >> 2) & 1) << 4);
           idx = ((2 + (kl >> 4)) * 64 + rho + 32 * ((kk >> 2) & 1)) * 8 + (((kk >> 3) << 2) | (kk & 3)); }
    kvf[(size_t)kt * 2048 + idx] = (bf16_t)(cvtpk(z ? s : s * QSCALE, 0.f) & 0xffffu);
    asm volatile("s_waitcnt vmcnt(4)" : "+v"(gw[0]), "+v"(gw[1]), "+v"(gc[0]), "+v"(gc[1]), "+v"(mq[0]), "+v"(mq[1]), "+v"(mq[2]), "+v"(mq[3]) :: "memory");
    float ga[8] = {0, 0, 0, 0, 0, 0, 0, 0};
#pragma unroll
    for (int i = 0; i < 2; ++i) { const float sv = silu_bf((bf16_t)gc[i]);
#pragma unroll
      for (int j = 0; j < 4; ++j) { ga[2 * j] += sv * bflo(gw[i][j]); ga[2 * j + 1] += sv * bfhi(gw[i][j]); } }
    float mv = rbf((__uint_as_float(mq[0]) + __uint_as_float(mq[1])) + (__uint_as_float(mq[2]) + __uint_as_float(mq[3]))); if (mi >= 3072) mv = rbf(1.f + mv);
    if (w == 7) { if (lane < 4) modf[1024 + mi] = mv; else if (lane >= 8 && lane < 16) cvec[ce] = (bf16_t)(__float_as_uint(mv) >> 16); }
#pragma unroll
    for (int j = 0; j < 8; ++j) ga[j] = sum4g(dpp_add<0x128>(ga[j]));
    float* gr = (float*)(smem + 34816 + 16384 + 8192);
    if (lane < 8) { *(f32x4*)(gr + w * 64 + lane * 8) = (f32x4){ga[0], ga[1], ga[2], ga[3]}; *(f32x4*)(gr + w * 64 + lane * 8 + 4) = (f32x4){ga[4], ga[5], ga[6], ga[7]}; }
    asm volatile("s_waitcnt lgkmcnt(0)\n\ts_barrier" ::: "memory");
    if (tid < 64) { float t = 0.f;
#pragma unroll
      for (int ww = 0; ww < 8; ++ww) t += gr[ww * 64 + tid];
      modp[16384 + gks * 2048 + gcg * 64 + tid] = t; }
  }
  ST(6);
  asm volatile("s_waitcnt vmcnt(0)" : "+v"(dummy));
  ST_END(4);
}

DI unsigned wimg_src_chunk(int lane) { return (unsigned)(((lane & 7) ^ (((lane >> 4) & 3) << 1)) << 4); }
DI int wimg_rd(int lane, int t) { const int g = lane >> 4, q = (lane & 15) >> 2, r8 = 4 * (g & 1) + q; return (g >> 1) * 1024 + r8 * 128 + ((t ^ (r8 >> 1)) * 32) + (lane & 3) * 8; }

DI bf16x8 pack8(const f32x16& p, int s) {
  u32x4 r = {cvtpk(p[8 * s], p[8 * s + 1]), cvtpk(p[8 * s + 2], p[8 * s + 3]), cvtpk(p[8 * s + 4], p[8 * s + 5]), cvtpk(p[8 * s + 6], p[8 * s + 7])};
  return __builtin_bit_cast(bf16x8, r);
}
DI float rowmax16(const f32x16& p) {
  float a = fmaxf(fmaxf(p[0], p[1]), p[2]), b = fmaxf(fmaxf(p[3], p[4]), p[5]);
  a = fmaxf(fmaxf(a, p[6]), p[7]); b = fmaxf(fmaxf(b, p[8]), p[9]); a = fmaxf(fmaxf(a, p[10]), p[11]); b = fmaxf(fmaxf(b, p[12]), p[13]);
  a = fmaxf(fmaxf(a, p[14]), p[15]); a = fmaxf(a, b);
  auto rr = __builtin_amdgcn_permlane32_swap(__float_as_uint(a), __float_as_uint(a), false, false);
  return fmaxf(__uint_as_float(rr[0]), __uint_as_float(rr[1]));
}
DI bf16x8 plo(const u32x8& p) { return __builtin_bit_cast(bf16x8, __builtin_shufflevector(p, p, 0, 1, 2, 3)); }
DI bf16x8 phi(const u32x8& p) { return __builtin_bit_cast(bf16x8, __builtin_shufflevector(p, p, 4, 5, 6, 7)); }
#define ROWSUM(sel, p, acc, idx) __builtin_amdgcn_smfmac_f32_16x16x64_bf16(__builtin_bit_cast(bf16x8n, sel), __builtin_bit_cast(bf16x16n, p), acc, idx, 0, 0)

__global__ __launch_bounds__(512, 2) void k3_attn(const bf16_t* hfm, const bf16_t* wq, const bf16_t* kvf, bf16_t* ofm, const bf16_t* wg, const bf16_t* wu,
                                                  const bf16_t* cvec, float* utp, const bf16_t* wo, const bf16_t* wd, const bf16_t* x,
                                                  const float* modp, float* modf STAMP_ARG) {
  extern __shared__ __attribute__((aligned(16))) char lds[];
  ST_DECL; ST(0);
  KEEP_S(hfm); KEEP_S(wq); KEEP_S(kvf); KEEP_S(ofm); KEEP_S(wg); KEEP_S(wu); KEEP_S(cvec); KEEP_S(utp); KEEP_S(wo); KEEP_S(wd); KEEP_S(x); KEEP_S(modp); KEEP_S(modf);
  ST(7);
  const int tid = threadIdx.x, lane = tid & 63, w = RFL(tid >> 6), wm = w & 3, wk = w >> 2;
  const int xcd = blockIdx.x & 7, bi = blockIdx.x >> 3, tm = (xcd >> 1) * 4 + (bi & 3), hp = (xcd & 1) * 8 + (bi >> 2);
  const unsigned lds0 = (unsigned)(uintptr_t)lds;
  const lds_cptr L = (lds_cptr)lds;
  const int hi = lane >> 5;
  const unsigned vlin = (unsigned)(lane * 16);
  constexpr int LW = 32768, LUT = 98304;
  const int rot = (bi >> 2) * 4, crot = bi & 7;
  u32x4 hf[32];
  const char* hb = (const char*)hfm + (size_t)(wk * 32 * 64 + tm * 4 + wm) * 1024;
#define HLOAD(i) GLOAD4(hf[i], hb + (size_t)((i) ^ rot) * 65536, vlin, 0)
#define KV_LD(c, slot, q) glds16((const char*)kvf + (((c) + crot) & 7) * 32768 + w * 4096 + (q) * 1024, vlin, lds0 + (slot) * 32768 + w * 4096 + (q) * 1024)
  const unsigned wvoff = (unsigned)((w * 8 + (lane >> 3)) * 2048 + hp * 128 + (((lane & 7) ^ (((lane >> 4) & 1) << 2)) << 4));
#define WLD(q) glds16((const char*)wq + ((q) ^ (rot >> 2)) * 64 * 2048, wvoff, lds0 + LW + ((q) * 8 + w) * 1024)
  WLD(0); WLD(1); WLD(8); WLD(9); HLOAD(0); HLOAD(1); HLOAD(2); HLOAD(3);
  u32x4 cf[8]; unsigned gpart;
  const unsigned cvoff = (unsigned)((lane >> 4) * 32 + (lane & 1) * 16), gvoff = (unsigned)(((lane >> 3) * 2048 + blockIdx.x * 8 + (lane & 7)) * 4);
#define CLOAD(J) GLOAD4(cf[J], (const char*)cvec + (xcd & 1) * 2048 + wk * 1024, cvoff, (J) * 128)
  f32x16 qa0 = {}, qa1 = {};
  {
    const int q = (lane & 15) >> 2, pp = lane & 3, nb = (lane >> 4) & 1;
    lds_cptr a0 = L + LW + wk * 65536 + hi * 1024 + q * 128 + (((q >> 1) & 1) * 64) + nb * 32 + pp * 8;
    lds_cptr a1 = L + LW + wk * 65536 + hi * 1024 + q * 128 + ((1 ^ ((q >> 1) & 1)) * 64) + nb * 32 + pp * 8;
    PIN2(a0, a1); PIN2(qa0, qa1);
    asm volatile("s_waitcnt vmcnt(4)\n\ts_barrier" ::: "memory");
    ST(1);
    bf16x8 w0 = ldtr2(a0, a0 + 512), w1 = ldtr2(a1, a1 + 512);
#define QSTEP(i, N, B, LOADS) do { asm volatile("s_waitcnt vmcnt(" #N ")" : "+v"(hf[i])); if ((i) == 23) WAITL(); if (B) BAR(); \
    bf16x8 n0 = w0, n1 = w1; if ((i) < 31) { n0 = ldtr2(a0 + ((i) + 1) * 2048, a0 + ((i) + 1) * 2048 + 512); n1 = ldtr2(a1 + ((i) + 1) * 2048, a1 + ((i) + 1) * 2048 + 512); } SBAR(); \
    qa0 = MFMA32(w0, __builtin_bit_cast(bf16x8, hf[i]), qa0); PIN(qa0); SBAR(); LOADS; qa1 = MFMA32(w1, __builtin_bit_cast(bf16x8, hf[i]), qa1); PIN(qa1); SBAR(); w0 = n0; w1 = n1; } while (0)
    QSTEP(0, 3, 0, HLOAD(4); WLD(2); HLOAD(5));
    QSTEP(1, 5, 0, WLD(3); HLOAD(6); WLD(10));
    QSTEP(2, 7, 0, WLD(11); HLOAD(7); HLOAD(8));
    QSTEP(3, 9, 0, HLOAD(9));
    QSTEP(4, 9, 0, HLOAD(10); WLD(4));
    QSTEP(5, 9, 0, HLOAD(11));
    QSTEP(6, 8, 0, WLD(5); HLOAD(12));
    QSTEP(7, 7, 1, WLD(12));
    QSTEP(8, 7, 0, HLOAD(13); WLD(13));
    QSTEP(9, 8, 0, HLOAD(14));
    QSTEP(10, 8, 0, HLOAD(15); HLOAD(16));
    QSTEP(11, 8, 0, HLOAD(17));
    QSTEP(12, 7, 0, HLOAD(18); WLD(6));
    QSTEP(13, 7, 0, HLOAD(19));
    QSTEP(14, 6, 0, WLD(7); HLOAD(20));
    QSTEP(15, 7, 1, WLD(14));
    QSTEP(16, 7, 0, HLOAD(21); WLD(15));
    QSTEP(17, 8, 0, HLOAD(22));
    QSTEP(18, 8, 0, HLOAD(23); HLOAD(24));
    QSTEP(19, 8, 0, HLOAD(25));
    QSTEP(20, 7, 0, KV_LD(0, 0, 0); HLOAD(26));
    QSTEP(21, 7, 0, HLOAD(27));
    QSTEP(22, 6, 0, KV_LD(0, 0, 1); HLOAD(28));
    QSTEP(23, 7, 1, HLOAD(29));
    QSTEP(24, 7, 0, KV_LD(0, 0, 2); HLOAD(30));
    QSTEP(25, 8, 0, HLOAD(31); KV_LD(0, 0, 3));
    QSTEP(26, 8, 0, CLOAD(0); CLOAD(1));
    QSTEP(27, 9, 0, CLOAD(2); CLOAD(3));
    QSTEP(28, 9, 0, CLOAD(4); CLOAD(5));
    QSTEP(29, 10, 0, CLOAD(6); CLOAD(7));
    QSTEP(30, 10, 0, GLOAD1(gpart, modp + 16384, gvoff));
    QSTEP(31, 10, 0, (void)0);
  }
  ST(2);
  bf16x8 qfa0, qfa1, qfb0, qfb1;
  {
    lds_ptr xw = (lds_ptr)lds + (w < 6 ? LUT + w * 8192 : 65536 + (w - 6) * 8192) + lane * 16;
    lds_cptr xr = L + ((w ^ 4) < 6 ? LUT + (w ^ 4) * 8192 : 65536 + ((w ^ 4) - 6) * 8192) + lane * 16;
#pragma unroll
    for (int i = 0; i < 4; ++i) { *(LAS f32x4*)(xw + i * 1024) = (f32x4){qa0[4 * i], qa0[4 * i + 1], qa0[4 * i + 2], qa0[4 * i + 3]};
      *(LAS f32x4*)(xw + 4096 + i * 1024) = (f32x4){qa1[4 * i], qa1[4 * i + 1], qa1[4 * i + 2], qa1[4 * i + 3]}; }
    KV_LD(1, 1, 0); KV_LD(1, 1, 1); KV_LD(1, 1, 2); KV_LD(1, 1, 3);
    asm volatile("s_waitcnt vmcnt(13) lgkmcnt(0)\n\ts_barrier" ::: "memory");
#pragma unroll
    for (int i = 0; i < 4; ++i) { const f32x4 o0 = *(const LAS f32x4*)(xr + i * 1024), o1 = *(const LAS f32x4*)(xr + 4096 + i * 1024);
#pragma unroll
      for (int j = 0; j < 4; ++j) { qa0[4 * i + j] += o0[j]; qa1[4 * i + j] += o1[j]; } }
    qfa0 = pack8(qa0, 0); qfa1 = pack8(qa0, 1); qfb0 = pack8(qa1, 0); qfb1 = pack8(qa1, 1);
  }
  u32x4 sel;
  { const unsigned on = (lane & 2) ? 0u : 0x3f803f80u; const bool od = (lane >> 2) & 1; sel = (u32x4){od ? 0u : on, od ? 0u : on, od ? on : 0u, od ? on : 0u}; }
  const int sidx = (lane & 1) ? 0xEEEE : 0x4444;
  f32x16 oa = {}, ob = {}; f32x4 la = {}, lb = {};
  const lds_cptr kv0 = L + wk * 16384 + lane * 16;
  bf16x8 kf0 = ld128(kv0), kf1 = ld128(kv0 + 1024), vf0 = ld128(kv0 + 2048), vf1 = vf0;
  f32x16 sA, sB, na, nb;
  {
    const f32x16 z = {};
    sA = MFMA32(kf0, qfa0, z); sB = MFMA32(kf0, qfb0, z); sA = MFMA32(kf1, qfa1, sA); sB = MFMA32(kf1, qfb1, sB);
    const float ma = rowmax16(sA), mb = rowmax16(sB);
#pragma unroll
    for (int r = 0; r < 16; ++r) { sA[r] -= ma; na[r] = -ma; nb[r] = -mb; }
    PIN2(na, nb);
  }
  u32x8 pA, pB = {0, 0, 0, 0, 0, 0, 0, 0};
  const int ub = bi < 28 ? bi : bi - 28, ucg = (xcd >> 1) * 14 + (ub % 14);
  const char* usrc = (const char*)(ub < 14 ? wg : wu) + ucg * 128 + (size_t)((xcd & 1) * 512 + wm * 8) * 7168;
  const unsigned uvoff = (unsigned)((lane >> 3) * 7168) + wimg_src_chunk(lane);
#define SIDE(cw, slot, k) do { if (!wk) { if ((cw) < 6) { const char* s_ = (const char*)kvf + (((cw) + 2 + crot) & 7) * 32768 + wm * 8192 + (k) * 2048; const unsigned d_ = lds0 + (slot) * 32768 + wm * 8192 + (k) * 2048; \
        glds16(s_, vlin, d_); glds16(s_ + 1024, vlin, d_ + 1024); } } \
    else if ((cw) < 4) glds16(usrc + (size_t)((cw) * 4 + (k)) * (32 * 7168), uvoff, lds0 + LUT + (((cw) * 4 + (k)) * 4 + wm) * 1024); \
    else if ((k) == 0) { if ((cw) == 4) TOUCH255((const char*)wd + (blockIdx.x * 4 + wm) * 7168, (unsigned)((lane < 56 ? lane : 55) * 128)); \
      else if ((cw) < 7) TOUCH255((const char*)wo + ((bi * 4 + wm) * 2 + (cw) - 5) * 8192, (unsigned)(lane * 128)); } \
    else if ((k) == 3 && (cw) == 5) TOUCH255((const char*)x + xcd * 524288 + (bi * 4 + wm) * 4096, (unsigned)((lane & 31) * 128)); } while (0)
#define EX(S, r) S[r] = __builtin_amdgcn_exp2f(S[r])
#define CV(P, S, r) P[(r) >> 1] = cvtpk(S[r], S[(r) + 1])
#define HS(SC, SN, PP, PC, q0, q1, nm, oac, lac, L0, L1, L2, L3, G) do { \
    SN = MFMA32(kf0, q0, nm); SBAR(); L0; EX(SC, 0); EX(SC, 1); EX(SC, 2); EX(SC, 3); PIN(SC); SBAR(); \
    SN = MFMA32(kf1, q1, SN); SBAR(); L1; EX(SC, 4); EX(SC, 5); CV(PC, SC, 0); CV(PC, SC, 2); EX(SC, 6); PIN2(SC, PC); SBAR(); \
    oac = MFMA32(vf0, plo(PP), oac); SBAR(); L2; EX(SC, 7); EX(SC, 8); CV(PC, SC, 4); EX(SC, 9); CV(PC, SC, 6); PIN2(SC, PC); SBAR(); \
    oac = MFMA32(vf1, phi(PP), oac); SBAR(); L3; EX(SC, 10); EX(SC, 11); CV(PC, SC, 8); EX(SC, 12); CV(PC, SC, 10); PIN2(SC, PC); SBAR(); \
    lac = ROWSUM(sel, PP, lac, sidx); SBAR(); G; EX(SC, 13); EX(SC, 14); EX(SC, 15); CV(PC, SC, 12); CV(PC, SC, 14); PIN(PC); SBAR(); } while (0)
#define STEP(pc, pn, G0, G1) do { HS(sA, sB, pB, pA, qfb0, qfb1, nb, ob, lb, kf0 = ld128(pn), kf1 = ld128((pn) + 1024), vf0 = ld128((pc) + 2048), vf1 = ld128((pc) + 3072), G0); \
    HS(sB, sA, pA, pB, qfa0, qfa1, na, oa, la, (void)0, (void)0, (void)0, (void)0, G1); } while (0)
  asm volatile("s_waitcnt vmcnt(4)" : "+v"(cf[0]), "+v"(cf[1]), "+v"(cf[2]), "+v"(cf[3]), "+v"(cf[4]), "+v"(cf[5]), "+v"(cf[6]), "+v"(cf[7]), "+v"(gpart) :: "memory");
  ST(3);
  int sl = 0;
#pragma unroll 1
  for (int c = 0; c < 8; ++c) {
    const int s1 = sl == 2 ? 0 : sl + 1, s2 = s1 == 2 ? 0 : s1 + 1;
    const lds_cptr pc = kv0 + sl * 32768, pn = kv0 + s1 * 32768;
    const unsigned cp = (unsigned)(c - 1);
    __builtin_amdgcn_s_setprio(2); STEP(pc, pc + 4096, SIDE(cp, s1, 2), SIDE(cp, s1, 3));
    __builtin_amdgcn_s_setprio(1); STEP(pc + 4096, pc + 8192, (void)0, (void)0);
    __builtin_amdgcn_s_setprio(0); STEP(pc + 8192, pc + 12288, (void)0, (void)0);
    if (c < 7) { if (!wk || c == 0 || c == 5) WAITV(0); BAR(); }
    __builtin_amdgcn_s_setprio(3);
    if (c == 5) {
      f32x4 ua = {};
      const lds_cptr wr = L + LUT + wk * 32768 + wimg_rd(lane, wm);
#pragma unroll
      for (int J = 0; J < 8; ++J) ua = MFMA16(ldtr2(wr + J * 4096, wr + J * 4096 + 2048), __builtin_bit_cast(bf16x8, cf[J]), ua);
      if ((lane & 15) < 2 && bi < 28) *(f32x4*)(utp + ((((xcd & 1) * 2 + wk) * 28 + (ucg >> 1)) * 4 + (ub < 14 ? 0 : 2) + (lane & 1)) * 128 + (ucg & 1) * 64 + 16 * wm + 4 * (lane >> 4)) = ua;
      const int gi = blockIdx.x * 8 + (lane & 7);
      const float t = sum4g(dpp_add<0x128>(__uint_as_float(gpart)));
      if (tid < 8) modf[(gi < 1024 ? 2048 : 4096) + gi] = rbf(t);
    }
    SBAR();
    STEP(pc + 12288, pn, SIDE(c, s2, 0), SIDE(c, s2, 1));
    sl = s1;
  }
  ob = MFMA32(vf0, plo(pB), ob); ob = MFMA32(vf1, phi(pB), ob); lb = ROWSUM(sel, pB, lb, sidx);
  ST(4);
  f32x16 oacc; float lsum; bool bad;
  {
    const f32x16 snd = wk ? oa : ob; oacc = wk ? ob : oa;
    const float l_a = la[0] + la[1], l_b = lb[0] + lb[1];
    const float ls = wk ? l_a : l_b, lk = wk ? l_b : l_a, ns = wk ? na[0] : nb[0], nk = wk ? nb[0] : na[0];
    lds_ptr xb = (lds_ptr)lds + LUT;
#pragma unroll
    for (int i = 0; i < 4; ++i) *(LAS f32x4*)(xb + w * 8192 + i * 1024 + lane * 16) = (f32x4){snd[4 * i], snd[4 * i + 1], snd[4 * i + 2], snd[4 * i + 3]};
    *(LAS f32x2*)(xb + w * 8192 + 4096 + lane * 8) = (f32x2){ls, ns};
    asm volatile("s_waitcnt lgkmcnt(0)\n\ts_barrier" ::: "memory");
    const f32x2 pl = *(const LAS f32x2*)(xb + (w ^ 4) * 8192 + 4096 + lane * 8);
    const float nmin = fminf(nk, pl[1]), fk = __builtin_amdgcn_exp2f(nmin - nk), fp = __builtin_amdgcn_exp2f(nmin - pl[1]);
    lsum = lk * fk + pl[0] * fp;
    bad = !(lk < 1.0e12f) || !(pl[0] < 1.0e12f);
#pragma unroll
    for (int i = 0; i < 4; ++i) { const f32x4 o = *(const LAS f32x4*)(xb + (w ^ 4) * 8192 + i * 1024 + lane * 16);
#pragma unroll
      for (int j = 0; j < 4; ++j) oacc[4 * i + j] = oacc[4 * i + j] * fk + o[j] * fp; }
  }
#pragma unroll
  for (int r = 0; r < 16; ++r) bad = bad || !(fabsf(oacc[r]) < 3.0e38f);
  if (__any(bad)) {
    const bf16x8 qf0 = wk ? qfb0 : qfa0, qf1 = wk ? qfb1 : qfa1;
    float m = -INFINITY; lsum = 0.f;
#pragma unroll
    for (int r = 0; r < 16; ++r) oacc[r] = 0.f;
    const bf16_t* g = kvf + lane * 8;
    for (int t = 0; t < 64; ++t) {
      const f32x16 z = {};
      f32x16 s = MFMA32(*(const bf16x8*)(g + t * 2048), qf0, z); s = MFMA32(*(const bf16x8*)(g + t * 2048 + 512), qf1, s);
      const float mn = fmaxf(m, rowmax16(s)), al = __builtin_amdgcn_exp2f(m - mn); m = mn;
#pragma unroll
      for (int r = 0; r < 16; ++r) { s[r] = __builtin_amdgcn_exp2f(s[r] - mn); oacc[r] *= al; }
      u32x8 p;
#pragma unroll
      for (int r = 0; r < 16; r += 2) CV(p, s, r);
      f32x4 lt = {}; lt = ROWSUM(sel, p, lt, sidx);
      lsum = lsum * al + (lt[0] + lt[1]);
      oacc = MFMA32(*(const bf16x8*)(g + t * 2048 + 1024), plo(p), oacc); oacc = MFMA32(*(const bf16x8*)(g + t * 2048 + 1536), phi(p), oacc);
    }
  }
  const float rl = __builtin_amdgcn_rcpf(lsum);
#pragma unroll
  for (int r = 0; r < 16; ++r) oacc[r] *= rl;
  {
    const int m16 = tm * 8 + wm * 2 + ((lane >> 4) & 1), J = hp * 2 + wk;
    bf16_t* op = ofm + ((size_t)(J * 128 + m16) * 64 + (lane & 15) + 32 * hi) * 8;
    store16(op, __builtin_bit_cast(u32x4, pack8(oacc, 0))); store16(op + 128, __builtin_bit_cast(u32x4, pack8(oacc, 1)));
  }
  ST(6);
  ST_END(5);
}

#define GLOAD2(dst, sbase, voff, imm) asm volatile("global_load_dwordx2 %0, %1, %2 offset:%3" : "=v"(dst) : "v"(voff), "s"(sbase), "n"(imm) : "memory")
__global__ __launch_bounds__(512, 2) void k4_oproj(const bf16_t* ofm, const bf16_t* wo, const bf16_t* x, const float* modf, bf16_t* x1fm, bf16_t* yfm, f32x2* stat,
                                                   const float* utp, float* utf, unsigned* flag STAMP_ARG) {
  extern __shared__ __attribute__((aligned(16))) char lds[];
  ST_DECL; ST(0);
  KEEP_S(ofm); KEEP_S(wo); KEEP_S(x); KEEP_S(modf); KEEP_S(x1fm); KEEP_S(yfm); KEEP_S(stat); KEEP_S(utp); KEEP_S(utf); KEEP_S(flag);
  ST(7);
  const int tid = threadIdx.x, lane = tid & 63, w = RFL(tid >> 6), wm = w & 3, wk = w >> 2;
  const int xcd = blockIdx.x & 7, bi = blockIdx.x >> 3, tm = xcd * 2 + (bi & 1), tn = bi >> 1;
  const unsigned lds0 = (unsigned)(uintptr_t)lds;
  const lds_cptr L = (lds_cptr)lds;
  const unsigned vlin = (unsigned)(lane * 16);
  u32x4 af[16][2];
  const char* ab = (const char*)ofm + (size_t)(wk * 16 * 128 + tm * 8 + wm * 2) * 1024;
#define AL(i, h) GLOAD4(af[i][h], ab + (size_t)((i) ^ tn) * 131072, vlin, (h) * 1024)
  const unsigned wvoff = (unsigned)(((w ^ ((tn & 1) << 2)) * 8 + (lane >> 3)) * 2048 + tn * 128) + wimg_src_chunk(lane);
#define OWL(q) glds16((const char*)wo + ((q) ^ (tn >> 1)) * 64 * 2048, wvoff, lds0 + ((q) * 8 + w) * 1024)
  AL(0, 0); AL(0, 1); OWL(0); OWL(1); OWL(8); OWL(9); AL(1, 0); AL(1, 1);
  const int g = lane >> 4, tok = tm * 128 + wm * 32 + wk * 16 + (lane & 15);
  const unsigned xvoff = (unsigned)((lane >> 3) * 2048 + (((lane & 7) ^ (lane >> 3)) << 4)), mvoff = (unsigned)(((lane >> 4) & 1) * 8192 + (lane & 15) * 16);
#define OEL(k) do { if ((k) < 2) glds16((const char*)x + (size_t)(tm * 128 + wm * 32 + wk * 16 + (k) * 8) * 2048 + tn * 128, xvoff, lds0 + 131072 + w * 4096 + (k) * 1024); \
    else glds16(modf + 2048 + tn * 64, mvoff, lds0 + 131072 + w * 4096 + 2048); } while (0)
  unsigned up[4]; const unsigned uvoff = (unsigned)(((blockIdx.x * 8 + w) * 7 + (lane < 7 ? lane : 0)) * 4);
#define UL(k) GLOAD1(up[k], utp + (k) * 14336, uvoff)
  f32x4 acc[2][4] = {};
  {
    lds_cptr a[4];
#pragma unroll
    for (int t = 0; t < 4; ++t) { a[t] = L + wk * 65536 + wimg_rd(lane, t); PIN(a[t]); PIN2(acc[0][t], acc[1][t]); }
    asm volatile("s_waitcnt vmcnt(2)\n\ts_barrier" ::: "memory");
    ST(1);
    bf16x8 wf[4], wn[4];
#pragma unroll
    for (int t = 0; t < 4; ++t) wf[t] = ldtr2(a[t], a[t] + 2048);
#define OMM(i, t) do { acc[0][t] = MFMA16(wf[t], __builtin_bit_cast(bf16x8, af[i][0]), acc[0][t]); acc[1][t] = MFMA16(wf[t], __builtin_bit_cast(bf16x8, af[i][1]), acc[1][t]); PIN2(acc[0][t], acc[1][t]); SBAR(); } while (0)
#define OSTEP(i, N, B, L0, L1, L2, L3) do { asm volatile("s_waitcnt vmcnt(" #N ")" : "+v"(af[i][0]), "+v"(af[i][1])); if (B) BAR(); \
    _Pragma("unroll") for (int t = 0; t < 4; ++t) wn[t] = (i) < 15 ? ldtr2(a[t] + ((i) + 1) * 4096, a[t] + ((i) + 1) * 4096 + 2048) : wf[t]; SBAR(); \
    OMM(i, 0); L0; OMM(i, 1); L1; OMM(i, 2); L2; OMM(i, 3); L3; _Pragma("unroll") for (int t = 0; t < 4; ++t) wf[t] = wn[t]; } while (0)
    OSTEP(0, 6, 0, AL(2, 0); AL(2, 1), OWL(2), OWL(3); OWL(10), OWL(11));
    OSTEP(1, 6, 0, AL(3, 0), AL(3, 1), AL(4, 0), AL(4, 1));
    OSTEP(2, 8, 0, OWL(4), AL(5, 0), AL(5, 1), OWL(5));
    OSTEP(3, 6, 1, OWL(12), AL(6, 0), AL(6, 1), (void)0);
    OSTEP(4, 7, 0, OWL(13), AL(7, 0), AL(7, 1), (void)0);
    OSTEP(5, 7, 0, OWL(6), AL(8, 0), AL(8, 1), (void)0);
    OSTEP(6, 6, 0, OWL(7), AL(9, 0), AL(9, 1), (void)0);
    OSTEP(7, 6, 1, OWL(14), AL(10, 0), AL(10, 1), (void)0);
    OSTEP(8, 6, 0, OWL(15), AL(11, 0), AL(11, 1), (void)0);
    OSTEP(9, 6, 0, OEL(0), AL(12, 0), AL(12, 1), (void)0);
    OSTEP(10, 6, 0, OEL(1), AL(13, 0), AL(13, 1), (void)0);
    OSTEP(11, 6, 1, OEL(2), AL(14, 0), AL(14, 1), (void)0);
    OSTEP(12, 6, 0, UL(0), AL(15, 0), AL(15, 1), (void)0);
    OSTEP(13, 6, 0, UL(1), UL(2), UL(3), (void)0);
    OSTEP(14, 6, 0, (void)0, (void)0, (void)0, (void)0);
    OSTEP(15, 3, 0, (void)0, (void)0, (void)0, (void)0);
  }
  ST(2);
  lds_ptr xb = (lds_ptr)lds + 131072;
  u32x2 xr[4]; f32x4 gav[4], cbv[4];
  asm volatile("s_waitcnt vmcnt(0)" : "+v"(up[0]), "+v"(up[1]), "+v"(up[2]), "+v"(up[3]) :: "memory");
  if (lane == 8) flag[(blockIdx.x * 8 + w) * 32] = 0u;
  if (lane < 7) utf[uvoff >> 2] = (__uint_as_float(up[0]) + __uint_as_float(up[1])) + (__uint_as_float(up[2]) + __uint_as_float(up[3]));
#pragma unroll
  for (int t = 0; t < 4; ++t) { const lds_cptr e = xb + w * 4096;
    xr[t] = *(const LAS u32x2*)(e + (lane & 15) * 128 + (((2 * t + (g >> 1)) ^ (lane & 7)) << 4) + (g & 1) * 8);
    gav[t] = *(const LAS f32x4*)(e + 2048 + t * 64 + g * 16); cbv[t] = *(const LAS f32x4*)(e + 2048 + 256 + t * 64 + g * 16); }
  asm volatile("s_waitcnt lgkmcnt(0)" : "+v"(xr[0]), "+v"(xr[1]), "+v"(xr[2]), "+v"(xr[3]), "+v"(gav[0]), "+v"(gav[1]), "+v"(gav[2]), "+v"(gav[3]),
               "+v"(cbv[0]), "+v"(cbv[1]), "+v"(cbv[2]), "+v"(cbv[3]) :: "memory");
#pragma unroll
  for (int t = 0; t < 4; ++t) *(LAS f32x4*)(xb + w * 4096 + t * 1024 + lane * 16) = wk ? acc[0][t] : acc[1][t];
  asm volatile("s_waitcnt lgkmcnt(0)\n\ts_barrier" ::: "memory");
  float v[16]; float s = 0.f;
#pragma unroll
  for (int t = 0; t < 4; ++t) {
    const f32x4 o = *(const LAS f32x4*)(xb + (w ^ 4) * 4096 + t * 1024 + lane * 16) + (wk ? acc[1][t] : acc[0][t]), ga = gav[t];
    v[4 * t] = rbf(bflo(xr[t][0]) + ga[0] * o[0]); v[4 * t + 1] = rbf(bfhi(xr[t][0]) + ga[1] * o[1]);
    v[4 * t + 2] = rbf(bflo(xr[t][1]) + ga[2] * o[2]); v[4 * t + 3] = rbf(bfhi(xr[t][1]) + ga[3] * o[3]);
    s += (v[4 * t] + v[4 * t + 1]) + (v[4 * t + 2] + v[4 * t + 3]);
  }
  const size_t fo = ((size_t)(tn * 2 * 128 + (tok >> 4)) * 64 + lane) * 8;
#pragma unroll
  for (int jj = 0; jj < 2; ++jj) {
    const float* p = v + 8 * jj; const f32x4 c0 = cbv[2 * jj], c1 = cbv[2 * jj + 1];
    store16(yfm + fo + jj * 65536, (u32x4){cvtpk(p[0] * c0[0], p[1] * c0[1]), cvtpk(p[2] * c0[2], p[3] * c0[3]), cvtpk(p[4] * c1[0], p[5] * c1[1]), cvtpk(p[6] * c1[2], p[7] * c1[3])});
    store16(x1fm + fo + jj * 65536, (u32x4){cvtpk(p[0], p[1]), cvtpk(p[2], p[3]), cvtpk(p[4], p[5]), cvtpk(p[6], p[7])});
  }
  const float mw = sum4g(s) * (1.f / 64.f); float q = 0.f;
#pragma unroll
  for (int j = 0; j < 16; ++j) { const float d = v[j] - mw; q += d * d; }
  q = sum4g(q);
  if (g == 0) stat[((tok >> 4) * 16 + tn) * 16 + (lane & 15)] = (f32x2){mw, q};
  ST(6);
  ST_END(3);
}

__global__ __launch_bounds__(512, 2) void k5_gateup(const bf16_t* yfm, const bf16_t* wg, const bf16_t* wu, const f32x2* stat, const float* utp, bf16_t* actfm STAMP_ARG) {
  extern __shared__ __attribute__((aligned(16))) char lds[];
  ST_DECL; ST(0);
  KEEP_S(yfm); KEEP_S(wg); KEEP_S(wu); KEEP_S(stat); KEEP_S(utp); KEEP_S(actfm);
  ST(7);
  const int tid = threadIdx.x, lane = tid & 63, w = RFL(tid >> 6), wm = w & 3, wn = w >> 2;
  const int xcd = blockIdx.x & 7, bi = blockIdx.x >> 3, tm = (xcd & 1) * 4 + (bi & 3), tn = (xcd >> 1) * 7 + (bi >> 2);
  const unsigned lds0 = (unsigned)(uintptr_t)lds;
  const lds_cptr L = (lds_cptr)lds;
  const char* sA = (const char*)yfm + (size_t)(tm * 16 + 2 * w) * 1024;
  const char* sB = (const char*)(w < 4 ? wg : wu) + (size_t)(tn * 128 + ((w >> 1) & 1) * 64) * 2 + (size_t)(16 * (w & 1)) * 7168;
  const unsigned vA = (unsigned)(lane * 16), vB = (unsigned)((lane >> 3) * 7168) + wimg_src_chunk(lane);
#define K5_ISSUE(s, slot) do { const unsigned d_ = lds0 + (slot) * 32768 + w * 2048; \
    glds16(sA + (size_t)(s) * 131072, vA, d_); glds16(sA + (size_t)(s) * 131072 + 1024, vA, d_ + 1024); \
    glds16(sB + (size_t)(s) * 32 * 7168, vB, d_ + 16384); glds16(sB + (size_t)(s) * 32 * 7168 + 8 * 7168, vB, d_ + 16384 + 1024); } while (0)
  { const char* sS = (const char*)stat + tm * 32768 + w * 4096; const unsigned d_ = lds0 + 3 * 32768 + w * 4096;
    glds16(sS, vA, d_); glds16(sS + 1024, vA, d_ + 1024); glds16(sS + 2048, vA, d_ + 2048); glds16(sS + 3072, vA, d_ + 3072); }
  glds16((const char*)utp + tn * 2048 + (w & 1) * 1024, vA, lds0 + 131072 + w * 1024);
  K5_ISSUE(0, 0); K5_ISSUE(1, 1);
  lds_cptr ra[2], rb[2][4];
#pragma unroll
  for (int h = 0; h < 2; ++h) { ra[h] = L + h * 65536 + wm * 4096 + lane * 16; PIN(ra[h]);
#pragma unroll
    for (int t = 0; t < 4; ++t) { rb[h][t] = L + h * 65536 + 16384 + wn * 4096 + wimg_rd(lane, t); PIN(rb[h][t]); } }
  asm volatile("s_waitcnt vmcnt(8)\n\ts_barrier" ::: "memory");
  K5_ISSUE(2, 2);
  f32x4 ag[4][4], au[4][4]; float rs[4];
  {
    float sd[4], nmean[4];
#pragma unroll
    for (int m = 0; m < 4; ++m) {
      const lds_cptr sp = L + 3 * 32768 + (wm * 4 + m) * 2048 + (lane >> 4) * 512 + (lane & 15) * 8;
      f32x2 st[4];
#pragma unroll
      for (int j = 0; j < 4; ++j) st[j] = *(const LAS f32x2*)(sp + j * 128);
      const float mean = sum4g((st[0][0] + st[1][0]) + (st[2][0] + st[3][0])) * (1.f / 16.f); float q = 0.f;
#pragma unroll
      for (int j = 0; j < 4; ++j) { const float d = st[j][0] - mean; q += st[j][1] + 64.f * d * d; }
      const float var = sum4g(q) * (1.f / D_) + LN_EPS;
      rs[m] = __builtin_amdgcn_rsqf(var); sd[m] = var * rs[m]; nmean[m] = -mean;
    }
#pragma unroll
    for (int t = 0; t < 4; ++t) { const LAS float* p = (const LAS float*)(L + 131072) + wn * 64 + t * 16 + 4 * (lane >> 4);
      const f32x4 ug = *(const LAS f32x4*)(p), tg = *(const LAS f32x4*)(p + 128), uu = *(const LAS f32x4*)(p + 256), tu = *(const LAS f32x4*)(p + 384);
#pragma unroll
      for (int m = 0; m < 4; ++m) { ag[m][t] = tg * sd[m] + ug * nmean[m]; au[m][t] = tu * sd[m] + uu * nmean[m]; PIN2(ag[m][t], au[m][t]); } }
  }
  asm volatile("s_waitcnt vmcnt(8) lgkmcnt(0)\n\ts_barrier" ::: "memory");
  ST(1);
  if (wn) BAR();
#define K5_STEP(slot, sn, LAST) do { \
    bf16x8 xa[4], wgf[4], wuf[4]; \
    _Pragma("unroll") for (int t = 0; t < 4; ++t) { const lds_cptr p_ = rb[(slot) >> 1][t] + ((slot) & 1) * 32768; wgf[t] = ldtr2(p_, p_ + 2048); wuf[t] = ldtr2(p_ + 8192, p_ + 8192 + 2048); } \
    _Pragma("unroll") for (int m = 0; m < 4; ++m) xa[m] = ld128(ra[(slot) >> 1] + ((slot) & 1) * 32768 + m * 1024); \
    if ((sn) < 32) { K5_ISSUE(sn, ((slot) + 3) & 3); WAITV(8); } else WAITV(0); \
    asm volatile("s_waitcnt lgkmcnt(0)\n\ts_barrier" ::: "memory"); SBAR(); \
    __builtin_amdgcn_s_setprio(1); \
    _Pragma("unroll") for (int m = 0; m < 4; ++m) _Pragma("unroll") for (int t = 0; t < 4; ++t) { ag[m][t] = MFMA16(wgf[t], xa[m], ag[m][t]); au[m][t] = MFMA16(wuf[t], xa[m], au[m][t]); } \
    __builtin_amdgcn_s_setprio(0); SBAR(); \
    if (!(LAST)) BAR(); } while (0)
#pragma unroll 1
  for (int s = 0; s < 32; s += 4) {
    K5_STEP(0, s + 3, false);
    K5_STEP(1, s + 4, false);
    K5_STEP(2, s + 5, false);
    K5_STEP(3, s + 6, s == 28 && wn);
  }
  ST(2);
#pragma unroll
  for (int m = 0; m < 4; ++m) {
    const float r2 = rs[m] * rs[m], k0 = -2.302208198f * rs[m], k1 = (0.044715f * k0) * r2, iv = __builtin_amdgcn_rcpf(r2);
#pragma unroll
    for (int jj = 0; jj < 2; ++jj) {
      u32x4 o;
#pragma unroll
      for (int e = 0; e < 4; ++e) {
        const f32x4 a4 = ag[m][2 * jj + (e >> 1)], u4 = au[m][2 * jj + (e >> 1)];
        const f32x2 a = {a4[2 * (e & 1)], a4[2 * (e & 1) + 1]}, u = {u4[2 * (e & 1)], u4[2 * (e & 1) + 1]};
        const f32x2 z = a * ((a * a) * k1 + k0);
        const f32x2 ex = {__builtin_amdgcn_exp2f(z[0]), __builtin_amdgcn_exp2f(z[1])};
        const f32x2 dn = ex * iv + iv;
        const f32x2 rc = {__builtin_amdgcn_rcpf(dn[0]), __builtin_amdgcn_rcpf(dn[1])};
        const f32x2 r = (a * u) * rc;
        o[e] = cvtpk(r[0], r[1]);
      }
      store16(actfm + ((size_t)((tn * 4 + wn * 2 + jj) * 128 + tm * 16 + wm * 4 + m) * 64 + lane) * 8, o);
      SBAR();
    }
  }
  ST(6);
  ST_END(3);
}

__global__ __launch_bounds__(512, 2) void k6_down(const bf16_t* actfm, const bf16_t* wd, const bf16_t* x1fm, const float* modf, bf16_t* out, unsigned* flag, float* part STAMP_ARG) {
  extern __shared__ __attribute__((aligned(16))) char lds[];
  ST_DECL; ST(0);
  KEEP_S(actfm); KEEP_S(wd); KEEP_S(x1fm); KEEP_S(modf); KEEP_S(out); KEEP_S(flag); KEEP_S(part);
  ST(7);
  const int tid = threadIdx.x, lane = tid & 63, w = RFL(tid >> 6), wm = w & 3, wk = w >> 2;
  const int xcd = blockIdx.x & 7, bi = blockIdx.x >> 3, z = xcd & 1, tm = (xcd >> 1) * 4 + (bi & 3), tn = (bi >> 2) * 2 + z;
  const unsigned lds0 = (unsigned)(uintptr_t)lds;
  const lds_cptr L = (lds_cptr)lds;
  constexpr int N1 = 22, KMID = N1 * 64, K2 = 3584 - KMID;
  const unsigned vW = (unsigned)((lane >> 3) * 2048) + wimg_src_chunk(lane), vA = (unsigned)(lane * 16);
  const char* w1 = (const char*)wd + (size_t)(z * K2 + w * 8) * 2048;
  const char* a1 = (const char*)actfm + ((size_t)(z * (K2 / 32) + wk) * 128 + tm * 8 + wm * 2) * 1024;
  const char* w2 = (const char*)wd + (size_t)(KMID + w * 16) * 2048 + tn * 128;
  const char* a2 = (const char*)actfm + ((size_t)(KMID / 32 + 2 * wk) * 128 + tm * 8 + wm * 2) * 1024;
  u32x4 ar[4][4];
#define D1W(u, g) glds16(w1 + 64 * 2048 + (tn ^ (g)) * 128, vW, lds0 + (u) * 16384 + (g) * 8192 + w * 1024)
#define D1A(u, h) GLOAD4(ar[u][h], a1, vA, (h) * 1024)
#define D1NEXT() do { w1 += 64 * 2048; a1 += 2 * 131072; } while (0)
#define D2W(u, j, h) glds16(w2 + ((j) * 128 + (h) * 8) * 2048, vW, lds0 + (u) * 16384 + w * 2048 + (h) * 1024)
#define D2A(u, j, k) GLOAD4(ar[u][k], a2 + (size_t)((j) * 4 + ((k) >> 1)) * 131072, vA, ((k) & 1) * 1024)
  w1 -= 64 * 2048; D1W(0, 0); D1W(0, 1); w1 += 64 * 2048;
  D1W(1, 0); D1W(1, 1); D1A(0, 0); D1A(0, 1); D1NEXT(); D1W(2, 0); D1W(2, 1); D1A(1, 0); D1A(1, 1); D1NEXT(); D1W(3, 0); D1W(3, 1); D1A(2, 0); D1A(2, 1);
  f32x4 acc[2][8] = {};
  lds_cptr a[4], a2l[4];
#pragma unroll
  for (int t = 0; t < 4; ++t) { a[t] = L + wk * 4096 + wimg_rd(lane, t); a2l[t] = a[t] + wk * 4096; PIN2(a[t], a2l[t]); }
#pragma unroll
  for (int t = 0; t < 8; ++t) PIN2(acc[0][t], acc[1][t]);
  bf16x8 wf[2][8];
#define DFR(u, M, t) do { if ((M) == 1) wf[(u) & 1][t] = ldtr2(a[(t) & 3] + (u) * 16384 + ((t) >> 2) * 8192, a[(t) & 3] + (u) * 16384 + ((t) >> 2) * 8192 + 2048); \
    else if ((M) == 2) wf[(u) & 1][t] = ldtr2(a2l[(t) & 3] + (u) * 16384 + ((t) >> 2) * 4096, a2l[(t) & 3] + (u) * 16384 + ((t) >> 2) * 4096 + 2048); } while (0)
#define DMM(u, CM, NM, t) do { DFR(((u) + 1) & 3, NM, t); SBAR(); constexpr int c_ = (CM) == 1 ? (t) : ((t) & 3), h_ = (CM) == 1 ? 0 : 2 * ((t) >> 2); \
    acc[0][c_] = MFMA16(wf[(u) & 1][t], __builtin_bit_cast(bf16x8, ar[u][h_]), acc[0][c_]); acc[1][c_] = MFMA16(wf[(u) & 1][t], __builtin_bit_cast(bf16x8, ar[u][h_ + 1]), acc[1][c_]); \
    PIN2(acc[0][c_], acc[1][c_]); SBAR(); } while (0)
#define DSTEP(u, N, CM, NM, PRE, L0, L1, L2, L3, L4, L5, L6, L7) do { \
    if ((CM) == 1) asm volatile("s_waitcnt vmcnt(" #N ") lgkmcnt(0)\n\ts_barrier" : "+v"(ar[u][0]), "+v"(ar[u][1]) :: "memory"); \
    else asm volatile("s_waitcnt vmcnt(" #N ") lgkmcnt(0)\n\ts_barrier" : "+v"(ar[u][0]), "+v"(ar[u][1]), "+v"(ar[u][2]), "+v"(ar[u][3]) :: "memory"); \
    PRE; SBAR(); \
    DMM(u, CM, NM, 0); L0; DMM(u, CM, NM, 1); L1; DMM(u, CM, NM, 2); L2; DMM(u, CM, NM, 3); L3; \
    DMM(u, CM, NM, 4); L4; DMM(u, CM, NM, 5); L5; DMM(u, CM, NM, 6); L6; DMM(u, CM, NM, 7); L7; } while (0)
#define PFD 0
  const char* tbase = lane < 32 ? (const char*)wd : (const char*)actfm;
  const unsigned t1o = lane < 32 ? (unsigned)(z * K2 * 2048 + bi * 4096 + lane * 128) : (unsigned)(((z * (K2 / 32) + (bi >> 4)) * 128 + (xcd >> 1) * 32) * 1024 + (bi & 15) * 2048 + (lane & 15) * 128);
  const unsigned t1s = lane < 32 ? 131072u : 262144u;
  const unsigned t2o = lane < 32 ? (unsigned)((KMID + bi * 4 + (lane >> 3)) * 2048 + (lane & 7) * 256 + z * 128) : (unsigned)(((KMID / 32 + (bi >> 3)) * 128 + (xcd >> 1) * 32) * 1024 + (bi & 7) * 4096 + (lane & 31) * 128);
#define DTOUCH(st) do { if (PFD && w == ((st) & 7) && (st) + PFD < N1 + 6) { const unsigned o_ = (st) + PFD < N1 ? t1o + (unsigned)((st) + PFD) * t1s : t2o + (unsigned)((st) + PFD - N1) * 2u * t1s; \
    asm volatile("global_load_dword v255, %0, off" :: "v"(tbase + o_) : "memory", "v255"); } } while (0)
#define D1STD(u) do { D1NEXT(); DSTEP(u, 8, 1, 1, (void)0, D1W(u, 0), (void)0, D1W(u, 1), (void)0, D1A(((u) + 3) & 3, 0), (void)0, D1A(((u) + 3) & 3, 1), DTOUCH(s + (u))); } while (0)
  asm volatile("s_waitcnt vmcnt(12)\n\ts_barrier" ::: "memory");
#pragma unroll
  for (int t = 0; t < 8; ++t) DFR(0, 1, t);
  lds_ptr xb = (lds_ptr)lds + 65536;
  f32x4 snd[4]; u32x4 rcv[4], xr[2]; unsigned fl;
  const int g = lane >> 4, tok = tm * 128 + wm * 32 + wk * 16 + (lane & 15);
  char* pmine = (char*)part + (size_t)(blockIdx.x * 8 + w) * 4096; const char* ptheir = (const char*)part + (size_t)((blockIdx.x ^ 1) * 8 + w) * 4096;
  unsigned* fmine = flag + (blockIdx.x * 8 + w) * 32; const unsigned* ftheir = flag + ((blockIdx.x ^ 1) * 8 + w) * 32;
#define DSEND_WRITE() do { _Pragma("unroll") for (int t = 0; t < 4; ++t) *(LAS f32x4*)(xb + w * 4096 + t * 1024 + lane * 16) = wk ? acc[0][4 + t] : acc[1][4 + t]; } while (0)
#define DSEND_READ() do { _Pragma("unroll") for (int t = 0; t < 4; ++t) snd[t] = *(const LAS f32x4*)(xb + (w ^ 4) * 4096 + t * 1024 + lane * 16) + (wk ? acc[1][4 + t] : acc[0][4 + t]); } while (0)
#define DSEND(k) asm volatile("global_store_dwordx4 %0, %1, %2 offset:%3 sc1\n\ts_nop 1" :: "v"(vA), "v"(snd[k]), "s"(pmine), "n"((k) * 1024) : "memory")
#define DFLAG_ST() asm volatile("global_store_dword %0, %1, %2 sc1" :: "v"(0u), "v"(1u), "s"(fmine) : "memory")
#define DFLAG_LD() asm volatile("global_load_dword %0, %1, %2 sc1" : "=v"(fl) : "v"(0u), "s"(ftheir) : "memory")
#define DRECV(k) asm volatile("global_load_dwordx4 %0, %1, %2 offset:%3 sc1" : "=v"(rcv[k]) : "v"(vA), "s"(ptheir), "n"((k) * 1024) : "memory")
#define DEL(k) do { if ((k) < 2) GLOAD4(xr[(k) & 1], (const char*)x1fm + (size_t)((tn * 2 + (k)) * 128 + tm * 8 + wm * 2 + wk) * 1024, vA, 0); \
    else glds16(modf + 5120 + tn * 64, (unsigned)((lane & 15) * 16), lds0 + 98304 + w * 1024); } while (0)
#pragma unroll 1
  for (int s = 0; s < 16; s += 4) { D1STD(0); D1STD(1); D1STD(2); D1STD(3); }
  ST(4);
  D1NEXT(); DSTEP(0, 8, 1, 1, (void)0, D1W(0, 0), (void)0, D1W(0, 1), (void)0, D1A(3, 0), (void)0, D1A(3, 1), DTOUCH(16));
  D1NEXT(); DSTEP(1, 8, 1, 1, (void)0, D1W(1, 0), (void)0, D1W(1, 1), (void)0, D1A(0, 0), (void)0, D1A(0, 1), DTOUCH(17));
  D1NEXT(); DSTEP(2, 8, 1, 1, (void)0, D2W(2, 0, 0), (void)0, D2W(2, 0, 1), (void)0, D1A(1, 0), (void)0, D1A(1, 1), DTOUCH(18));
  DSTEP(3, 8, 1, 1, (void)0, D2W(3, 1, 0), D2W(3, 1, 1), D2A(2, 0, 0), (void)0, D2A(2, 0, 1), D2A(2, 0, 2), D2A(2, 0, 3), DTOUCH(19));
  DSTEP(0, 10, 1, 1, (void)0, D2W(0, 2, 0), D2W(0, 2, 1), D2A(3, 1, 0), (void)0, D2A(3, 1, 1), D2A(3, 1, 2), D2A(3, 1, 3), DTOUCH(20));
  DSTEP(1, 12, 1, 2, (void)0, D2W(1, 3, 0), D2W(1, 3, 1), D2A(0, 2, 0), (void)0, D2A(0, 2, 1), D2A(0, 2, 2), D2A(0, 2, 3), DTOUCH(21));
  ST(5);
  DSEND_WRITE();
  DSTEP(2, 12, 2, 2, DSEND_READ(), DSEND(0); DSEND(1), DSEND(2), DSEND(3), D2W(2, 4, 0), D2W(2, 4, 1); D2A(1, 3, 0), D2A(1, 3, 1), D2A(1, 3, 2), D2A(1, 3, 3));
  DSTEP(3, 16, 2, 2, (void)0, D2W(3, 5, 0), D2W(3, 5, 1), D2A(2, 4, 0), (void)0, D2A(2, 4, 1), D2A(2, 4, 2), D2A(2, 4, 3), (void)0);
  DSTEP(0, 12, 2, 2, (void)0, DFLAG_ST(), D2A(3, 5, 0), (void)0, D2A(3, 5, 1), D2A(3, 5, 2), (void)0, D2A(3, 5, 3), (void)0);
  DSTEP(1, 11, 2, 2, (void)0, DEL(0), (void)0, DEL(1), (void)0, (void)0, DEL(2), (void)0, (void)0);
  DSTEP(2, 8, 2, 2, (void)0, DFLAG_LD(), (void)0, (void)0, (void)0, (void)0, (void)0, (void)0, (void)0);
  DSTEP(3, 0, 2, 0, (void)0, DRECV(0), (void)0, DRECV(1), (void)0, DRECV(2), (void)0, DRECV(3), (void)0);
  ST(2);
#pragma unroll
  for (int t = 0; t < 4; ++t) *(LAS f32x4*)(xb + w * 4096 + t * 1024 + lane * 16) = wk ? acc[0][t] : acc[1][t];
  asm volatile("s_waitcnt vmcnt(0) lgkmcnt(0)\n\ts_barrier" : "+v"(xr[0]), "+v"(xr[1]), "+v"(rcv[0]), "+v"(rcv[1]), "+v"(rcv[2]), "+v"(rcv[3]), "+v"(fl) :: "memory");
  if (RFL(fl) == 0u) {
    for (int it = 0; it < (1 << 22); ++it) { if (RFL(__hip_atomic_load(ftheir, __ATOMIC_RELAXED, __HIP_MEMORY_SCOPE_AGENT)) != 0u) break; __builtin_amdgcn_s_sleep(2); }
    asm volatile("global_load_dwordx4 %0, %4, %5 sc1\n\tglobal_load_dwordx4 %1, %4, %5 offset:1024 sc1\n\tglobal_load_dwordx4 %2, %4, %5 offset:2048 sc1\n\t"
                 "global_load_dwordx4 %3, %4, %5 offset:3072 sc1\n\ts_waitcnt vmcnt(0)" : "=&v"(rcv[0]), "=&v"(rcv[1]), "=&v"(rcv[2]), "=&v"(rcv[3]) : "v"(vA), "s"(ptheir) : "memory");
  }
#pragma unroll
  for (int jj = 0; jj < 2; ++jj) {
    unsigned pk[2][2];
#pragma unroll
    for (int tt = 0; tt < 2; ++tt) { const int t = 2 * jj + tt;
      const f32x4 o = (*(const LAS f32x4*)(xb + (w ^ 4) * 4096 + t * 1024 + lane * 16) + (wk ? acc[1][t] : acc[0][t])) + __builtin_bit_cast(f32x4, rcv[t]);
      const f32x4 gb = *(const LAS f32x4*)(L + 98304 + w * 1024 + t * 64 + g * 16);
      pk[tt][0] = cvtpk(bflo(xr[jj][2 * tt]) + gb[0] * o[0], bfhi(xr[jj][2 * tt]) + gb[1] * o[1]);
      pk[tt][1] = cvtpk(bflo(xr[jj][2 * tt + 1]) + gb[2] * o[2], bfhi(xr[jj][2 * tt + 1]) + gb[3] * o[3]); }
    auto r0 = __builtin_amdgcn_permlane16_swap(pk[0][0], pk[1][0], false, false);
    auto r1 = __builtin_amdgcn_permlane16_swap(pk[0][1], pk[1][1], false, false);
    *(u32x4*)(out + (size_t)tok * D_ + tn * 64 + jj * 32 + (g & 1) * 16 + (g >> 1) * 8) = (u32x4){r0[0], r1[0], r0[1], r1[1]};
  }
  ST(6);
  ST_END(3);
}

extern "C" void kernel_launch(void* const* d_in, const int* in_sizes, int n_in, void* d_out, int out_size, void* d_ws, size_t ws_size, hipStream_t stream) {
  const bf16_t* x = (const bf16_t*)d_in[0]; const bf16_t* wq = (const bf16_t*)d_in[1]; const bf16_t* wk = (const bf16_t*)d_in[2];
  const bf16_t* wv = (const bf16_t*)d_in[3]; const bf16_t* wo = (const bf16_t*)d_in[4]; const bf16_t* wg = (const bf16_t*)d_in[6];
  const bf16_t* wu = (const bf16_t*)d_in[7]; const bf16_t* wd = (const bf16_t*)d_in[8]; const bf16_t* c = (const bf16_t*)d_in[9];
  const bf16_t* wmod = (const bf16_t*)d_in[10];
  char* ws = (char*)d_ws;
  float* modp = (float*)(ws + WS_MODP); float* modf = (float*)(ws + WS_MODF); bf16_t* cvec = (bf16_t*)(ws + WS_CVEC);
  bf16_t* wkvf = (bf16_t*)(ws + WS_WKVF); bf16_t* kvf = (bf16_t*)(ws + WS_KVF);
  float* utp = (float*)(ws + WS_UTP); float* utf = (float*)(ws + WS_UTF); unsigned* flag = (unsigned*)(ws + WS_FLAG); float* part = (float*)(ws + WS_PART); f32x2* stat = (f32x2*)(ws + WS_STAT);
  bf16_t* hfm = (bf16_t*)(ws + WS_H); bf16_t* ofm = (bf16_t*)(ws + WS_O); bf16_t* x1fm = (bf16_t*)(ws + WS_X1); bf16_t* yfm = (bf16_t*)(ws + WS_Y); bf16_t* actfm = (bf16_t*)(ws + WS_ACT);
  (void)hipFuncSetAttribute((const void*)k3_attn, hipFuncAttributeMaxDynamicSharedMemorySize, 163840);
  (void)hipFuncSetAttribute((const void*)k4_oproj, hipFuncAttributeMaxDynamicSharedMemorySize, 163840);
  (void)hipFuncSetAttribute((const void*)k5_gateup, hipFuncAttributeMaxDynamicSharedMemorySize, 139264);
  (void)hipFuncSetAttribute((const void*)k6_down, hipFuncAttributeMaxDynamicSharedMemorySize, 106496);
  k1_mod<<<288, 256, 0, stream>>>(c, wmod, wk, wv, modp, wkvf, x STAMP_PASS(0));
  k2_ln_kv<<<256, 512, 0, stream>>>(x, modp, wkvf, wq, hfm, kvf, modf, cvec, c, wmod STAMP_PASS(1));
  k3_attn<<<256, 512, 163840, stream>>>(hfm, wq, kvf, ofm, wg, wu, cvec, utp, wo, wd, x, modp, modf STAMP_PASS(2));
  k4_oproj<<<256, 512, 163840, stream>>>(ofm, wo, x, modf, x1fm, yfm, stat, utp, utf, flag STAMP_PASS(3));
  k5_gateup<<<224, 512, 139264, stream>>>(yfm, wg, wu, stat, utf, actfm STAMP_PASS(4));
  k6_down<<<256, 512, 106496, stream>>>(actfm, wd, x1fm, modf, (bf16_t*)d_out, flag, part STAMP_PASS(5));
}
```

```cpp
#include <hip/hip_runtime.h>
#include <stdint.h>

typedef unsigned short bf16_t;
typedef short bf16x8 __attribute__((ext_vector_type(8)));
typedef short s16x4 __attribute__((ext_vector_type(4)));
typedef float f32x2 __attribute__((ext_vector_type(2)));
typedef float f32x4 __attribute__((ext_vector_type(4)));
typedef float f32x16 __attribute__((ext_vector_type(16)));
typedef unsigned u32x2 __attribute__((ext_vector_type(2)));
typedef unsigned u32x4 __attribute__((ext_vector_type(4)));
typedef __bf16 bf16x2_t __attribute__((ext_vector_type(2)));
#define LAS __attribute__((address_space(3)))
typedef LAS const char* lds_cptr;
typedef LAS char* lds_ptr;
#define DI __device__ __forceinline__

constexpr int S_ = 2048, D_ = 1024, F_ = 3584, NMOD = 6144;
constexpr float LN_EPS = 1e-5f;
constexpr float QSCALE = 0.17677669529663687f * 1.4426950408889634f;

constexpr size_t WS_MODP = 0;
constexpr size_t WS_MODF = 128 * 1024;
constexpr size_t WS_CVEC = 160 * 1024;
constexpr size_t WS_WKVF = 192 * 1024;
constexpr size_t WS_KVF  = 384 * 1024;
constexpr size_t WS_UTF  = 640 * 1024;
constexpr size_t WS_UTP  = 32u << 20;
constexpr size_t WS_STAT = 768 * 1024;
constexpr size_t WS_H    = 1u << 20;
constexpr size_t WS_O    = 5u << 20;
constexpr size_t WS_X1   = 9u << 20;
constexpr size_t WS_Y    = 13u << 20;
constexpr size_t WS_ACT  = 17u << 20;

DI unsigned cvtpk(float lo, float hi) { f32x2 v = {lo, hi}; bf16x2_t b = __builtin_convertvector(v, bf16x2_t); return __builtin_bit_cast(unsigned, b); }
DI float bflo(unsigned u) { return __uint_as_float(u << 16); }
DI float bfhi(unsigned u) { return __uint_as_float(u & 0xffff0000u); }
DI float rbf(float x) { return bflo(cvtpk(x, 0.f) & 0xffffu); }
DI float bf2f(bf16_t b) { return __uint_as_float((unsigned)b << 16); }
DI void glds16(const void* sbase, unsigned voff, unsigned lds_dst) {
  unsigned keep;
  asm volatile("s_mov_b32 %0, m0\n\ts_mov_b32 m0, %3\n\ts_nop 0\n\tglobal_load_lds_dwordx4 %1, %2\n\ts_mov_b32 m0, %0"
               : "=&s"(keep) : "v"(voff), "s"(sbase), "s"(lds_dst) : "memory");
}
#define GLOAD4(dst, sbase, voff, imm) asm volatile("global_load_dwordx4 %0, %1, %2 offset:%3" : "=v"(dst) : "v"(voff), "s"(sbase), "n"(imm) : "memory")
#define GLOAD4NT(dst, sbase, voff) asm volatile("global_load_dwordx4 %0, %1, %2 nt" : "=v"(dst) : "v"(voff), "s"(sbase) : "memory")
#define GLOAD1(dst, sbase, voff) asm volatile("global_load_dword %0, %1, %2" : "=v"(dst) : "v"(voff), "s"(sbase) : "memory")
#define GLOADH(dst, sbase, voff) asm volatile("global_load_ushort %0, %1, %2" : "=v"(dst) : "v"(voff), "s"(sbase) : "memory")
typedef short v4i16_t __attribute__((ext_vector_type(4)));
DI s16x4 ldtr(lds_cptr p) { return __builtin_bit_cast(s16x4, __builtin_amdgcn_ds_read_tr16_b64_v4i16((LAS v4i16_t*)p)); }
DI bf16x8 ldtr2(lds_cptr p0, lds_cptr p1) { s16x4 a = ldtr(p0), b = ldtr(p1); return (bf16x8){a[0], a[1], a[2], a[3], b[0], b[1], b[2], b[3]}; }
DI bf16x8 ld128(lds_cptr p) { return *(const LAS bf16x8*)p; }
#define MFMA32(a, b, c) __builtin_amdgcn_mfma_f32_32x32x16_bf16(a, b, c, 0, 0, 0)
#define MFMA16(a, b, c) __builtin_amdgcn_mfma_f32_16x16x32_bf16(a, b, c, 0, 0, 0)
#define SBAR() __builtin_amdgcn_sched_barrier(0)
#define BAR() asm volatile("s_barrier" ::: "memory")
#define WAITV(n) asm volatile("s_waitcnt vmcnt(" #n ")" ::: "memory")
#define WAITL() asm volatile("s_waitcnt lgkmcnt(0)" ::: "memory")
#define RFL(x) __builtin_amdgcn_readfirstlane(x)
#define PIN(x) asm volatile("" : "+v"(x))
#define PIN2(x, y) asm volatile("" : "+v"(x), "+v"(y))
#define WT 1
DI void store16(void* p, u32x4 v) {
  asm volatile("global_store_dwordx4 %0, %1, off sc1\n\ts_nop 1" :: "v"(p), "v"(v) : "memory");
}
DI float sum4g(float x) {
  auto a = __builtin_amdgcn_permlane16_swap(__float_as_uint(x), __float_as_uint(x), false, false); x = __uint_as_float(a[0]) + __uint_as_float(a[1]);
  auto b = __builtin_amdgcn_permlane32_swap(__float_as_uint(x), __float_as_uint(x), false, false); return __uint_as_float(b[0]) + __uint_as_float(b[1]);
}
template <int CTRL> DI float dpp_add(float x) { return x + __int_as_float(__builtin_amdgcn_update_dpp(0, __float_as_int(x), CTRL, 0xf, 0xf, true)); }
DI float sum64(float x) { x = dpp_add<0xB1>(x); x = dpp_add<0x4E>(x); x = dpp_add<0x141>(x); x = dpp_add<0x140>(x); return sum4g(x); }
#define KEEP_S(p) asm volatile("" :: "s"(p))
#define TOUCH(dummy, sbase, voff) asm volatile("global_load_dword %0, %1, %2" : "+v"(dummy) : "v"(voff), "s"(sbase) : "memory")
#define TOUCH255(sbase, voff) asm volatile("global_load_dword v255, %0, %1" :: "v"(voff), "s"(sbase) : "memory", "v255")
typedef __bf16 bf16x8n __attribute__((ext_vector_type(8)));
typedef __bf16 bf16x16n __attribute__((ext_vector_type(16)));
typedef unsigned u32x8 __attribute__((ext_vector_type(8)));

#define STAMP_ARG
#define STAMP_PASS(k)
#define ST_DECL
#define ST(i)
#define ST_END(i)

#define XTOUCH 1
DI float silu_bf(bf16_t c) { const float cv = bf2f(c); return rbf(cv * __builtin_amdgcn_rcpf(1.f + __expf(-cv))); }
__global__ __launch_bounds__(256, 2) void k1_mod(const bf16_t* __restrict__ c, const bf16_t* __restrict__ wmod, const bf16_t* __restrict__ wk,
                                                 const bf16_t* __restrict__ wv, float* __restrict__ modp, bf16_t* __restrict__ wkvf, const bf16_t* __restrict__ x STAMP_ARG) {
  __shared__ float red[4][64];
  ST_DECL; ST(0);
  KEEP_S(c); KEEP_S(wmod); KEEP_S(wk); KEEP_S(wv); KEEP_S(modp); KEEP_S(wkvf); KEEP_S(x);
  const int b = blockIdx.x, tid = threadIdx.x;
  ST(1);
  if (__builtin_expect(b < 256, 1)) {
    const int cg = b & 63, ks = b >> 6, l8 = tid & 7, r = tid >> 3;
    const bf16_t* wp = wmod + (size_t)(ks * 256 + r) * NMOD + cg * 64 + (cg >= 32 ? 1024 : 0) + l8 * 8;
    u32x4 w[8]; bf16_t cr[8];
#pragma unroll
    for (int i = 0; i < 8; ++i) w[i] = __builtin_nontemporal_load((const u32x4*)(wp + (size_t)i * 32 * NMOD));
#pragma unroll
    for (int i = 0; i < 8; ++i) cr[i] = c[ks * 256 + i * 32 + r];
    SBAR();
    unsigned dummy = 0;
    if (XTOUCH) TOUCH(dummy, x, (unsigned)((b & 7) * 524288 + ((b >> 3) * 128 + (tid >> 6) * 32 + (tid & 31)) * 128));
    ST(3);
    float acc[8] = {0, 0, 0, 0, 0, 0, 0, 0};
#pragma unroll
    for (int i = 0; i < 8; ++i) {
      const float sv = silu_bf(cr[i]);
#pragma unroll
      for (int j = 0; j < 4; ++j) { acc[2 * j] += sv * bflo(w[i][j]); acc[2 * j + 1] += sv * bfhi(w[i][j]); }
    }
#pragma unroll
    for (int j = 0; j < 8; ++j) acc[j] = sum4g(dpp_add<0x128>(acc[j]));
    if ((tid & 63) < 8) { *(f32x4*)&red[tid >> 6][l8 * 8] = (f32x4){acc[0], acc[1], acc[2], acc[3]}; *(f32x4*)&red[tid >> 6][l8 * 8 + 4] = (f32x4){acc[4], acc[5], acc[6], acc[7]}; }
    __syncthreads();
    ST(6);
    if (tid < 64) modp[ks * 4096 + cg * 64 + tid] = (red[0][tid] + red[1][tid]) + (red[2][tid] + red[3][tid]);
    asm volatile("s_waitcnt vmcnt(0)" : "+v"(dummy));
  } else {
    const int it = (b - 256) * 256 + tid, lane = it & 63, ct = (it >> 6) & 1, J = (it >> 7) & 31, z = it >> 12;
    const bf16_t* src = (z ? wv : wk) + (size_t)(32 * J + 8 * (lane >> 4)) * 32 + ct * 16 + (lane & 15);
    unsigned o[4];
#pragma unroll
    for (int j = 0; j < 4; ++j) o[j] = (unsigned)src[(2 * j) * 32] | ((unsigned)src[(2 * j + 1) * 32] << 16);
    *(u32x4*)(wkvf + (size_t)it * 8) = (u32x4){o[0], o[1], o[2], o[3]};
  }
  ST_END(7);
}

__global__ __launch_bounds__(512, 2) void k2_ln_kv(const bf16_t* __restrict__ x, float* __restrict__ modp, const bf16_t* __restrict__ wkvf,
                                                   const bf16_t* __restrict__ wq, bf16_t* __restrict__ hfm, bf16_t* __restrict__ kvf,
                                                   float* __restrict__ modf, bf16_t* __restrict__ cvec, const bf16_t* __restrict__ c, const bf16_t* __restrict__ wmod STAMP_ARG) {
  __shared__ __attribute__((aligned(16))) char smem[34816 + 16384 + 8192 + 2048];
  ST_DECL; ST(0);
  KEEP_S(x); KEEP_S(modp); KEEP_S(wkvf); KEEP_S(wq); KEEP_S(hfm); KEEP_S(kvf); KEEP_S(modf); KEEP_S(cvec); KEEP_S(c); KEEP_S(wmod);
  ST(7);
  const int tid = threadIdx.x, lane = tid & 63, w = RFL(tid >> 6);
  const int xcd = blockIdx.x & 7, bi = blockIdx.x >> 3, z = bi & 1, tt = xcd * 16 + (bi >> 1);
  u32x4 xv[2][2];
#pragma unroll
  for (int rr = 0; rr < 2; ++rr)
#pragma unroll
    for (int hh = 0; hh < 2; ++hh) xv[rr][hh] = *(const u32x4*)(x + (size_t)(tt * 16 + 2 * w + rr) * D_ + hh * 512 + lane * 8);
  f32x4 mp[4];
#pragma unroll
  for (int p = 0; p < 4; ++p) mp[p] = *(const f32x4*)(modp + p * 4096 + tid * 4);
  SBAR();
  float* mt = (float*)(smem + 34816 + 16384);
  float v[2][16], rstd[2];
#pragma unroll
  for (int rr = 0; rr < 2; ++rr)
#pragma unroll
    for (int hh = 0; hh < 2; ++hh)
#pragma unroll
      for (int j = 0; j < 4; ++j) { v[rr][hh * 8 + 2 * j] = bflo(xv[rr][hh][j]); v[rr][hh * 8 + 2 * j + 1] = bfhi(xv[rr][hh][j]); }
  u32x4 wf[8];
  const char* wfp = (const char*)wkvf + (z * 64 + w * 8) * 1024; const unsigned vlin = (unsigned)(lane * 16);
  { f32x4 a = (mp[0] + mp[1]) + (mp[2] + mp[3]);
#pragma unroll
    for (int j = 0; j < 4; ++j) { a[j] = rbf(a[j]); if (w >= 4) a[j] = rbf(1.f + a[j]); }
    *(f32x4*)(mt + tid * 4) = a; }
  { float s0 = 0.f, s1 = 0.f;
#pragma unroll
    for (int j = 0; j < 16; ++j) { s0 += v[0][j]; s1 += v[1][j]; }
    PIN2(s0, s1); SBAR();
#pragma unroll
    for (int i = 0; i < 4; ++i) GLOAD4(wf[i], wfp, vlin, i * 1024);
    SBAR();
    s0 = sum64(s0) * (1.f / D_); s1 = sum64(s1) * (1.f / D_);
    float q0 = 0.f, q1 = 0.f;
#pragma unroll
    for (int j = 0; j < 16; ++j) { v[0][j] -= s0; q0 += v[0][j] * v[0][j]; v[1][j] -= s1; q1 += v[1][j] * v[1][j]; }
    rstd[0] = __builtin_amdgcn_rsqf(sum64(q0) * (1.f / D_) + LN_EPS); rstd[1] = __builtin_amdgcn_rsqf(sum64(q1) * (1.f / D_) + LN_EPS); }
  PIN2(rstd[0], rstd[1]);
  asm volatile("s_waitcnt lgkmcnt(0)\n\ts_barrier" ::: "memory");
  ST(1);
#pragma unroll
  for (int i = 4; i < 8; ++i) GLOAD4(wf[i], wfp + 4096, vlin, (i - 4) * 1024);
  const int gcg = blockIdx.x & 31, gks = blockIdx.x >> 5;
  u32x4 gw[2]; unsigned gc[2], mq[4], dummy;
#pragma unroll
  for (int i = 0; i < 2; ++i) { GLOAD4NT(gw[i], wmod + (size_t)(gks * 128 + i * 64) * NMOD + (gcg < 16 ? 2048 : 4096) + gcg * 64, (unsigned)((tid >> 3) * (NMOD * 2) + (tid & 7) * 16));
    GLOADH(gc[i], c + gks * 128 + i * 64, (unsigned)((tid >> 3) * 2)); }
  const int ce = blockIdx.x * 8 + (lane & 7);
  const int mi = (lane & 8) ? (((ce >> 3) & 1) ? 2048 : 3072) + 32 * (ce >> 6) + 16 * ((ce & 7) >> 2) + 4 * ((ce >> 4) & 3) + (ce & 3) : 3072 + blockIdx.x * 4 + (lane & 3);
#pragma unroll
  for (int p = 0; p < 4; ++p) GLOAD1(mq[p], modp + p * 4096, (unsigned)(mi * 4));
  { const int ln = bi * 256 + w * 32 + (lane & 31); GLOAD1(dummy, wq, (unsigned)((ln >> 3) * 2048 + (xcd & 1) * 1024 + (ln & 7) * 128)); }
  SBAR();
#pragma unroll
  for (int hh = 0; hh < 2; ++hh) {
    const f32x4 s0 = *(const f32x4*)(mt + hh * 512 + lane * 8), s1 = *(const f32x4*)(mt + hh * 512 + lane * 8 + 4);
    const f32x4 c0 = *(const f32x4*)(mt + 1024 + hh * 512 + lane * 8), c1 = *(const f32x4*)(mt + 1024 + hh * 512 + lane * 8 + 4);
#pragma unroll
    for (int rr = 0; rr < 2; ++rr) {
      const float* p = &v[rr][hh * 8]; const float r = rstd[rr];
      const u32x4 o = {cvtpk(p[0] * r * c0[0] + s0[0], p[1] * r * c0[1] + s0[1]), cvtpk(p[2] * r * c0[2] + s0[2], p[3] * r * c0[3] + s0[3]),
                       cvtpk(p[4] * r * c1[0] + s1[0], p[5] * r * c1[1] + s1[1]), cvtpk(p[6] * r * c1[2] + s1[2], p[7] * r * c1[3] + s1[3])};
      *(u32x4*)(smem + (hh * 64 + lane) * 272 + (2 * w + rr) * 16) = o;
    }
  }
  asm volatile("s_waitcnt vmcnt(9) lgkmcnt(0)\n\ts_barrier" : "+v"(wf[0]), "+v"(wf[1]), "+v"(wf[2]), "+v"(wf[3]), "+v"(wf[4]), "+v"(wf[5]), "+v"(wf[6]), "+v"(wf[7]) :: "memory");
  ST(2);
  f32x4 acc[2] = {};
#pragma unroll
  for (int jj = 0; jj < 4; ++jj) {
    const bf16x8 a = *(const bf16x8*)(smem + ((w * 4 + jj) * 4 + (lane >> 4)) * 272 + (lane & 15) * 16);
    acc[0] = MFMA16(a, __builtin_bit_cast(bf16x8, wf[jj * 2]), acc[0]); acc[1] = MFMA16(a, __builtin_bit_cast(bf16x8, wf[jj * 2 + 1]), acc[1]);
  }
  float* red = (float*)(smem + 34816);
  *(f32x4*)(red + ((w * 2) * 64 + lane) * 4) = acc[0]; *(f32x4*)(red + ((w * 2 + 1) * 64 + lane) * 4) = acc[1];
#pragma unroll
  for (int i = 0; i < 2; ++i) { const int it = i * 512 + tid, tok = it & 15, ch = z * 64 + (it >> 4);
    store16(hfm + ((size_t)((ch >> 1) * 64 + (tt >> 1)) * 64 + (tt & 1) * 16 + tok + 32 * (ch & 1)) * 8, *(const u32x4*)(smem + ch * 272 + tok * 16)); }
  asm volatile("s_waitcnt lgkmcnt(0)\n\ts_barrier" ::: "memory");
  ST(3);
  {
    float s = 0.f;
#pragma unroll
    for (int ww = 0; ww < 8; ++ww) s += red[ww * 512 + tid];
    const int r = tid & 3, ln = (tid >> 2) & 63, ct = tid >> 8;
    const int key = tt * 16 + 4 * (ln >> 4) + r, d = ct * 16 + (ln & 15), kt = key >> 5, kl = key & 31;
    int idx;
    if (z == 0) { const int dd = d & 15; idx = ((d >> 4) * 64 + kl + 32 * ((dd >> 2) & 1)) * 8 + (((dd >> 3) << 2) | (dd & 3)); }
    else { const int kk = kl & 15, rho = (d & 3) | (((d >> 3) & 1) << 2) | (((d >> 4) & 1) << 3) | (((d >> 2) & 1) << 4);
           idx = ((2 + (kl >> 4)) * 64 + rho + 32 * ((kk >> 2) & 1)) * 8 + (((kk >> 3) << 2) | (kk & 3)); }
    kvf[(size_t)kt * 2048 + idx] = (bf16_t)(cvtpk(z ? s : s * QSCALE, 0.f) & 0xffffu);
    asm volatile("s_waitcnt vmcnt(4)" : "+v"(gw[0]), "+v"(gw[1]), "+v"(gc[0]), "+v"(gc[1]), "+v"(mq[0]), "+v"(mq[1]), "+v"(mq[2]), "+v"(mq[3]) :: "memory");
    float ga[8] = {0, 0, 0, 0, 0, 0, 0, 0};
#pragma unroll
    for (int i = 0; i < 2; ++i) { const float sv = silu_bf((bf16_t)gc[i]);
#pragma unroll
      for (int j = 0; j < 4; ++j) { ga[2 * j] += sv * bflo(gw[i][j]); ga[2 * j + 1] += sv * bfhi(gw[i][j]); } }
    float mv = rbf((__uint_as_float(mq[0]) + __uint_as_float(mq[1])) + (__uint_as_float(mq[2]) + __uint_as_float(mq[3]))); if (mi >= 3072) mv = rbf(1.f + mv);
    if (w == 7) { if (lane < 4) modf[1024 + mi] = mv; else if (lane >= 8 && lane < 16) cvec[ce] = (bf16_t)(__float_as_uint(mv) >> 16); }
#pragma unroll
    for (int j = 0; j < 8; ++j) ga[j] = sum4g(dpp_add<0x128>(ga[j]));
    float* gr = (float*)(smem + 34816 + 16384 + 8192);
    if (lane < 8) { *(f32x4*)(gr + w * 64 + lane * 8) = (f32x4){ga[0], ga[1], ga[2], ga[3]}; *(f32x4*)(gr + w * 64 + lane * 8 + 4) = (f32x4){ga[4], ga[5], ga[6], ga[7]}; }
    asm volatile("s_waitcnt lgkmcnt(0)\n\ts_barrier" ::: "memory");
    if (tid < 64) { float t = 0.f;
#pragma unroll
      for (int ww = 0; ww < 8; ++ww) t += gr[ww * 64 + tid];
      modp[16384 + gks * 2048 + gcg * 64 + tid] = t; }
  }
  ST(6);
  asm volatile("s_waitcnt vmcnt(0)" : "+v"(dummy));
  ST_END(4);
}

DI unsigned wimg_src_chunk(int lane) { return (unsigned)(((lane & 7) ^ (((lane >> 4) & 3) << 1)) << 4); }
DI int wimg_rd(int lane, int t) { const int g = lane >> 4, q = (lane & 15) >> 2, r8 = 4 * (g & 1) + q; return (g >> 1) * 1024 + r8 * 128 + ((t ^ (r8 >> 1)) * 32) + (lane & 3) * 8; }

DI bf16x8 pack8(const f32x16& p, int s) {
  u32x4 r = {cvtpk(p[8 * s], p[8 * s + 1]), cvtpk(p[8 * s + 2], p[8 * s + 3]), cvtpk(p[8 * s + 4], p[8 * s + 5]), cvtpk(p[8 * s + 6], p[8 * s + 7])};
  return __builtin_bit_cast(bf16x8, r);
}
DI float rowmax16(const f32x16& p) {
  float a = fmaxf(fmaxf(p[0], p[1]), p[2]), b = fmaxf(fmaxf(p[3], p[4]), p[5]);
  a = fmaxf(fmaxf(a, p[6]), p[7]); b = fmaxf(fmaxf(b, p[8]), p[9]); a = fmaxf(fmaxf(a, p[10]), p[11]); b = fmaxf(fmaxf(b, p[12]), p[13]);
  a = fmaxf(fmaxf(a, p[14]), p[15]); a = fmaxf(a, b);
  auto rr = __builtin_amdgcn_permlane32_swap(__float_as_uint(a), __float_as_uint(a), false, false);
  return fmaxf(__uint_as_float(rr[0]), __uint_as_float(rr[1]));
}
DI bf16x8 plo(const u32x8& p) { return __builtin_bit_cast(bf16x8, __builtin_shufflevector(p, p, 0, 1, 2, 3)); }
DI bf16x8 phi(const u32x8& p) { return __builtin_bit_cast(bf16x8, __builtin_shufflevector(p, p, 4, 5, 6, 7)); }
#define ROWSUM(sel, p, acc, idx) __builtin_amdgcn_smfmac_f32_16x16x64_bf16(__builtin_bit_cast(bf16x8n, sel), __builtin_bit_cast(bf16x16n, p), acc, idx, 0, 0)

__global__ __launch_bounds__(512, 2) void k3_attn(const bf16_t* hfm, const bf16_t* wq, const bf16_t* kvf, bf16_t* ofm, const bf16_t* wg, const bf16_t* wu,
                                                  const bf16_t* cvec, float* utp, const bf16_t* wo, const bf16_t* wd, const bf16_t* x,
                                                  const float* modp, float* modf STAMP_ARG) {
  extern __shared__ __attribute__((aligned(16))) char lds[];
  ST_DECL; ST(0);
  KEEP_S(hfm); KEEP_S(wq); KEEP_S(kvf); KEEP_S(ofm); KEEP_S(wg); KEEP_S(wu); KEEP_S(cvec); KEEP_S(utp); KEEP_S(wo); KEEP_S(wd); KEEP_S(x); KEEP_S(modp); KEEP_S(modf);
  ST(7);
  const int tid = threadIdx.x, lane = tid & 63, w = RFL(tid >> 6), wm = w & 3, wk = w >> 2;
  const int xcd = blockIdx.x & 7, bi = blockIdx.x >> 3, tm = (xcd >> 1) * 4 + (bi & 3), hp = (xcd & 1) * 8 + (bi >> 2);
  const unsigned lds0 = (unsigned)(uintptr_t)lds;
  const lds_cptr L = (lds_cptr)lds;
  const int hi = lane >> 5;
  const unsigned vlin = (unsigned)(lane * 16);
  constexpr int LW = 32768, LUT = 98304;
  const int rot = (bi >> 2) * 4, crot = bi & 7;
  u32x4 hf[32];
  const char* hb = (const char*)hfm + (size_t)(wk * 32 * 64 + tm * 4 + wm) * 1024;
#define HLOAD(i) GLOAD4(hf[i], hb + (size_t)((i) ^ rot) * 65536, vlin, 0)
#define KV_LD(c, slot, q) glds16((const char*)kvf + (((c) + crot) & 7) * 32768 + w * 4096 + (q) * 1024, vlin, lds0 + (slot) * 32768 + w * 4096 + (q) * 1024)
  const unsigned wvoff = (unsigned)((w * 8 + (lane >> 3)) * 2048 + hp * 128 + (((lane & 7) ^ (((lane >> 4) & 1) << 2)) << 4));
#define WLD(q) glds16((const char*)wq + ((q) ^ (rot >> 2)) * 64 * 2048, wvoff, lds0 + LW + ((q) * 8 + w) * 1024)
  WLD(0); WLD(1); WLD(8); WLD(9); HLOAD(0); HLOAD(1); HLOAD(2); HLOAD(3);
  u32x4 cf[8]; unsigned gpart;
  const unsigned cvoff = (unsigned)((lane >> 4) * 32 + (lane & 1) * 16), gvoff = (unsigned)(((lane >> 3) * 2048 + blockIdx.x * 8 + (lane & 7)) * 4);
#define CLOAD(J) GLOAD4(cf[J], (const char*)cvec + (xcd & 1) * 2048 + wk * 1024, cvoff, (J) * 128)
  f32x16 qa0 = {}, qa1 = {};
  {
    const int q = (lane & 15) >> 2, pp = lane & 3, nb = (lane >> 4) & 1;
    lds_cptr a0 = L + LW + wk * 65536 + hi * 1024 + q * 128 + (((q >> 1) & 1) * 64) + nb * 32 + pp * 8;
    lds_cptr a1 = L + LW + wk * 65536 + hi * 1024 + q * 128 + ((1 ^ ((q >> 1) & 1)) * 64) + nb * 32 + pp * 8;
    PIN2(a0, a1); PIN2(qa0, qa1);
    asm volatile("s_waitcnt vmcnt(4)\n\ts_barrier" ::: "memory");
    ST(1);
    bf16x8 w0 = ldtr2(a0, a0 + 512), w1 = ldtr2(a1, a1 + 512);
#define QSTEP(i, N, B, LOADS) do { asm volatile("s_waitcnt vmcnt(" #N ")" : "+v"(hf[i])); if ((i) == 23) WAITL(); if (B) BAR(); \
    bf16x8 n0 = w0, n1 = w1; if ((i) < 31) { n0 = ldtr2(a0 + ((i) + 1) * 2048, a0 + ((i) + 1) * 2048 + 512); n1 = ldtr2(a1 + ((i) + 1) * 2048, a1 + ((i) + 1) * 2048 + 512); } SBAR(); \
    qa0 = MFMA32(w0, __builtin_bit_cast(bf16x8, hf[i]), qa0); PIN(qa0); SBAR(); LOADS; qa1 = MFMA32(w1, __builtin_bit_cast(bf16x8, hf[i]), qa1); PIN(qa1); SBAR(); w0 = n0; w1 = n1; } while (0)
    QSTEP(0, 3, 0, HLOAD(4); WLD(2); HLOAD(5));
    QSTEP(1, 5, 0, WLD(3); HLOAD(6); WLD(10));
    QSTEP(2, 7, 0, WLD(11); HLOAD(7); HLOAD(8));
    QSTEP(3, 9, 0, HLOAD(9));
    QSTEP(4, 9, 0, HLOAD(10); WLD(4));
    QSTEP(5, 9, 0, HLOAD(11));
    QSTEP(6, 8, 0, WLD(5); HLOAD(12));
    QSTEP(7, 7, 1, WLD(12));
    QSTEP(8, 7, 0, HLOAD(13); WLD(13));
    QSTEP(9, 8, 0, HLOAD(14));
    QSTEP(10, 8, 0, HLOAD(15); HLOAD(16));
    QSTEP(11, 8, 0, HLOAD(17));
    QSTEP(12, 7, 0, HLOAD(18); WLD(6));
    QSTEP(13, 7, 0, HLOAD(19));
    QSTEP(14, 6, 0, WLD(7); HLOAD(20));
    QSTEP(15, 7, 1, WLD(14));
    QSTEP(16, 7, 0, HLOAD(21); WLD(15));
    QSTEP(17, 8, 0, HLOAD(22));
    QSTEP(18, 8, 0, HLOAD(23); HLOAD(24));
    QSTEP(19, 8, 0, HLOAD(25));
    QSTEP(20, 7, 0, KV_LD(0, 0, 0); HLOAD(26));
    QSTEP(21, 7, 0, HLOAD(27));
    QSTEP(22, 6, 0, KV_LD(0, 0, 1); HLOAD(28));
    QSTEP(23, 7, 1, HLOAD(29));
    QSTEP(24, 7, 0, KV_LD(0, 0, 2); HLOAD(30));
    QSTEP(25, 8, 0, HLOAD(31); KV_LD(0, 0, 3));
    QSTEP(26, 8, 0, CLOAD(0); CLOAD(1));
    QSTEP(27, 9, 0, CLOAD(2); CLOAD(3));
    QSTEP(28, 9, 0, CLOAD(4); CLOAD(5));
    QSTEP(29, 10, 0, CLOAD(6); CLOAD(7));
    QSTEP(30, 10, 0, GLOAD1(gpart, modp + 16384, gvoff));
    QSTEP(31, 10, 0, (void)0);
  }
  ST(2);
  bf16x8 qfa0, qfa1, qfb0, qfb1;
  {
    lds_ptr xw = (lds_ptr)lds + (w < 6 ? LUT + w * 8192 : 65536 + (w - 6) * 8192) + lane * 16;
    lds_cptr xr = L + ((w ^ 4) < 6 ? LUT + (w ^ 4) * 8192 : 65536 + ((w ^ 4) - 6) * 8192) + lane * 16;
#pragma unroll
    for (int i = 0; i < 4; ++i) { *(LAS f32x4*)(xw + i * 1024) = (f32x4){qa0[4 * i], qa0[4 * i + 1], qa0[4 * i + 2], qa0[4 * i + 3]};
      *(LAS f32x4*)(xw + 4096 + i * 1024) = (f32x4){qa1[4 * i], qa1[4 * i + 1], qa1[4 * i + 2], qa1[4 * i + 3]}; }
    KV_LD(1, 1, 0); KV_LD(1, 1, 1); KV_LD(1, 1, 2); KV_LD(1, 1, 3);
    asm volatile("s_waitcnt vmcnt(13) lgkmcnt(0)\n\ts_barrier" ::: "memory");
#pragma unroll
    for (int i = 0; i < 4; ++i) { const f32x4 o0 = *(const LAS f32x4*)(xr + i * 1024), o1 = *(const LAS f32x4*)(xr + 4096 + i * 1024);
#pragma unroll
      for (int j = 0; j < 4; ++j) { qa0[4 * i + j] += o0[j]; qa1[4 * i + j] += o1[j]; } }
    qfa0 = pack8(qa0, 0); qfa1 = pack8(qa0, 1); qfb0 = pack8(qa1, 0); qfb1 = pack8(qa1, 1);
  }
  u32x4 sel;
  { const unsigned on = (lane & 2) ? 0u : 0x3f803f80u; const bool od = (lane >> 2) & 1; sel = (u32x4){od ? 0u : on, od ? 0u : on, od ? on : 0u, od ? on : 0u}; }
  const int sidx = (lane & 1) ? 0xEEEE : 0x4444;
  f32x16 oa = {}, ob = {}; f32x4 la = {}, lb = {};
  const lds_cptr kv0 = L + wk * 16384 + lane * 16;
  bf16x8 kf0 = ld128(kv0), kf1 = ld128(kv0 + 1024), vf0 = ld128(kv0 + 2048), vf1 = vf0;
  f32x16 sA, sB, na, nb;
  {
    const f32x16 z = {};
    sA = MFMA32(kf0, qfa0, z); sB = MFMA32(kf0, qfb0, z); sA = MFMA32(kf1, qfa1, sA); sB = MFMA32(kf1, qfb1, sB);
    const float ma = rowmax16(sA), mb = rowmax16(sB);
#pragma unroll
    for (int r = 0; r < 16; ++r) { sA[r] -= ma; na[r] = -ma; nb[r] = -mb; }
    PIN2(na, nb);
  }
  u32x8 pA, pB = {0, 0, 0, 0, 0, 0, 0, 0};
  const int ub = bi < 28 ? bi : bi - 28, ucg = (xcd >> 1) * 14 + (ub % 14);
  const char* usrc = (const char*)(ub < 14 ? wg : wu) + ucg * 128 + (size_t)((xcd & 1) * 512 + wm * 8) * 7168;
  const unsigned uvoff = (unsigned)((lane >> 3) * 7168) + wimg_src_chunk(lane);
#define SIDE(cw, slot, k) do { if (!wk) { if ((cw) < 6) { const char* s_ = (const char*)kvf + (((cw) + 2 + crot) & 7) * 32768 + wm * 8192 + (k) * 2048; const unsigned d_ = lds0 + (slot) * 32768 + wm * 8192 + (k) * 2048; \
        glds16(s_, vlin, d_); glds16(s_ + 1024, vlin, d_ + 1024); } } \
    else if ((cw) < 4) glds16(usrc + (size_t)((cw) * 4 + (k)) * (32 * 7168), uvoff, lds0 + LUT + (((cw) * 4 + (k)) * 4 + wm) * 1024); \
    else if ((k) == 0) { if ((cw) == 4) TOUCH255((const char*)wd + (blockIdx.x * 4 + wm) * 7168, (unsigned)((lane < 56 ? lane : 55) * 128)); \
      else if ((cw) < 7) TOUCH255((const char*)wo + ((bi * 4 + wm) * 2 + (cw) - 5) * 8192, (unsigned)(lane * 128)); } \
    else if ((k) == 3 && (cw) == 5) TOUCH255((const char*)x + xcd * 524288 + (bi * 4 + wm) * 4096, (unsigned)((lane & 31) * 128)); } while (0)
#define EX(S, r) S[r] = __builtin_amdgcn_exp2f(S[r])
#define CV(P, S, r) P[(r) >> 1] = cvtpk(S[r], S[(r) + 1])
#define HS(SC, SN, PP, PC, q0, q1, nm, oac, lac, L0, L1, L2, L3, G) do { \
    SN = MFMA32(kf0, q0, nm); SBAR(); L0; EX(SC, 0); EX(SC, 1); EX(SC, 2); EX(SC, 3); PIN(SC); SBAR(); \
    SN = MFMA32(kf1, q1, SN); SBAR(); L1; EX(SC, 4); EX(SC, 5); CV(PC, SC, 0); CV(PC, SC, 2); EX(SC, 6); PIN2(SC, PC); SBAR(); \
    oac = MFMA32(vf0, plo(PP), oac); SBAR(); L2; EX(SC, 7); EX(SC, 8); CV(PC, SC, 4); EX(SC, 9); CV(PC, SC, 6); PIN2(SC, PC); SBAR(); \
    oac = MFMA32(vf1, phi(PP), oac); SBAR(); L3; EX(SC, 10); EX(SC, 11); CV(PC, SC, 8); EX(SC, 12); CV(PC, SC, 10); PIN2(SC, PC); SBAR(); \
    lac = ROWSUM(sel, PP, lac, sidx); SBAR(); G; EX(SC, 13); EX(SC, 14); EX(SC, 15); CV(PC, SC, 12); CV(PC, SC, 14); PIN(PC); SBAR(); } while (0)
#define STEP(pc, pn, G0, G1) do { HS(sA, sB, pB, pA, qfb0, qfb1, nb, ob, lb, kf0 = ld128(pn), kf1 = ld128((pn) + 1024), vf0 = ld128((pc) + 2048), vf1 = ld128((pc) + 3072), G0); \
    HS(sB, sA, pA, pB, qfa0, qfa1, na, oa, la, (void)0, (void)0, (void)0, (void)0, G1); } while (0)
  asm volatile("s_waitcnt vmcnt(4)" : "+v"(cf[0]), "+v"(cf[1]), "+v"(cf[2]), "+v"(cf[3]), "+v"(cf[4]), "+v"(cf[5]), "+v"(cf[6]), "+v"(cf[7]), "+v"(gpart) :: "memory");
  ST(3);
  int sl = 0;
#pragma unroll 1
  for (int c = 0; c < 8; ++c) {
    const int s1 = sl == 2 ? 0 : sl + 1, s2 = s1 == 2 ? 0 : s1 + 1;
    const lds_cptr pc = kv0 + sl * 32768, pn = kv0 + s1 * 32768;
    const unsigned cp = (unsigned)(c - 1);
    __builtin_amdgcn_s_setprio(2); STEP(pc, pc + 4096, SIDE(cp, s1, 2), SIDE(cp, s1, 3));
    __builtin_amdgcn_s_setprio(1); STEP(pc + 4096, pc + 8192, (void)0, (void)0);
    __builtin_amdgcn_s_setprio(0); STEP(pc + 8192, pc + 12288, (void)0, (void)0);
    if (c < 7) { if (!wk || c == 0 || c == 5) WAITV(0); BAR(); }
    __builtin_amdgcn_s_setprio(3);
    if (c == 5) {
      f32x4 ua = {};
      const lds_cptr wr = L + LUT + wk * 32768 + wimg_rd(lane, wm);
#pragma unroll
      for (int J = 0; J < 8; ++J) ua = MFMA16(ldtr2(wr + J * 4096, wr + J * 4096 + 2048), __builtin_bit_cast(bf16x8, cf[J]), ua);
      if ((lane & 15) < 2 && bi < 28) *(f32x4*)(utp + ((((xcd & 1) * 2 + wk) * 28 + (ucg >> 1)) * 4 + (ub < 14 ? 0 : 2) + (lane & 1)) * 128 + (ucg & 1) * 64 + 16 * wm + 4 * (lane >> 4)) = ua;
      const int gi = blockIdx.x * 8 + (lane & 7);
      const float t = sum4g(dpp_add<0x128>(__uint_as_float(gpart)));
      if (tid < 8) modf[(gi < 1024 ? 2048 : 4096) + gi] = rbf(t);
    }
    SBAR();
    STEP(pc + 12288, pn, SIDE(c, s2, 0), SIDE(c, s2, 1));
    sl = s1;
  }
  ob = MFMA32(vf0, plo(pB), ob); ob = MFMA32(vf1, phi(pB), ob); lb = ROWSUM(sel, pB, lb, sidx);
  ST(4);
  f32x16 oacc; float lsum; bool bad;
  {
    const f32x16 snd = wk ? oa : ob; oacc = wk ? ob : oa;
    const float l_a = la[0] + la[1], l_b = lb[0] + lb[1];
    const float ls = wk ? l_a : l_b, lk = wk ? l_b : l_a, ns = wk ? na[0] : nb[0], nk = wk ? nb[0] : na[0];
    lds_ptr xb = (lds_ptr)lds + LUT;
#pragma unroll
    for (int i = 0; i < 4; ++i) *(LAS f32x4*)(xb + w * 8192 + i * 1024 + lane * 16) = (f32x4){snd[4 * i], snd[4 * i + 1], snd[4 * i + 2], snd[4 * i + 3]};
    *(LAS f32x2*)(xb + w * 8192 + 4096 + lane * 8) = (f32x2){ls, ns};
    asm volatile("s_waitcnt lgkmcnt(0)\n\ts_barrier" ::: "memory");
    const f32x2 pl = *(const LAS f32x2*)(xb + (w ^ 4) * 8192 + 4096 + lane * 8);
    const float nmin = fminf(nk, pl[1]), fk = __builtin_amdgcn_exp2f(nmin - nk), fp = __builtin_amdgcn_exp2f(nmin - pl[1]);
    lsum = lk * fk + pl[0] * fp;
    bad = !(lk < 1.0e12f) || !(pl[0] < 1.0e12f);
#pragma unroll
    for (int i = 0; i < 4; ++i) { const f32x4 o = *(const LAS f32x4*)(xb + (w ^ 4) * 8192 + i * 1024 + lane * 16);
#pragma unroll
      for (int j = 0; j < 4; ++j) oacc[4 * i + j] = oacc[4 * i + j] * fk + o[j] * fp; }
  }
#pragma unroll
  for (int r = 0; r < 16; ++r) bad = bad || !(fabsf(oacc[r]) < 3.0e38f);
  if (__any(bad)) {
    const bf16x8 qf0 = wk ? qfb0 : qfa0, qf1 = wk ? qfb1 : qfa1;
    float m = -INFINITY; lsum = 0.f;
#pragma unroll
    for (int r = 0; r < 16; ++r) oacc[r] = 0.f;
    const bf16_t* g = kvf + lane * 8;
    for (int t = 0; t < 64; ++t) {
      const f32x16 z = {};
      f32x16 s = MFMA32(*(const bf16x8*)(g + t * 2048), qf0, z); s = MFMA32(*(const bf16x8*)(g + t * 2048 + 512), qf1, s);
      const float mn = fmaxf(m, rowmax16(s)), al = __builtin_amdgcn_exp2f(m - mn); m = mn;
#pragma unroll
      for (int r = 0; r < 16; ++r) { s[r] = __builtin_amdgcn_exp2f(s[r] - mn); oacc[r] *= al; }
      u32x8 p;
#pragma unroll
      for (int r = 0; r < 16; r += 2) CV(p, s, r);
      f32x4 lt = {}; lt = ROWSUM(sel, p, lt, sidx);
      lsum = lsum * al + (lt[0] + lt[1]);
      oacc = MFMA32(*(const bf16x8*)(g + t * 2048 + 1024), plo(p), oacc); oacc = MFMA32(*(const bf16x8*)(g + t * 2048 + 1536), phi(p), oacc);
    }
  }
  const float rl = __builtin_amdgcn_rcpf(lsum);
#pragma unroll
  for (int r = 0; r < 16; ++r) oacc[r] *= rl;
  {
    const int m16 = tm * 8 + wm * 2 + ((lane >> 4) & 1), J = hp * 2 + wk;
    bf16_t* op = ofm + ((size_t)(J * 128 + m16) * 64 + (lane & 15) + 32 * hi) * 8;
    store16(op, __builtin_bit_cast(u32x4, pack8(oacc, 0))); store16(op + 128, __builtin_bit_cast(u32x4, pack8(oacc, 1)));
  }
  ST(6);
  ST_END(5);
}

#define GLOAD2(dst, sbase, voff, imm) asm volatile("global_load_dwordx2 %0, %1, %2 offset:%3" : "=v"(dst) : "v"(voff), "s"(sbase), "n"(imm) : "memory")
__global__ __launch_bounds__(512, 2) void k4_oproj(const bf16_t* ofm, const bf16_t* wo, const bf16_t* x, const float* modf, bf16_t* x1fm, bf16_t* yfm, f32x2* stat,
                                                   const float* utp, float* utf STAMP_ARG) {
  extern __shared__ __attribute__((aligned(16))) char lds[];
  ST_DECL; ST(0);
  KEEP_S(ofm); KEEP_S(wo); KEEP_S(x); KEEP_S(modf); KEEP_S(x1fm); KEEP_S(yfm); KEEP_S(stat); KEEP_S(utp); KEEP_S(utf);
  ST(7);
  const int tid = threadIdx.x, lane = tid & 63, w = RFL(tid >> 6), wm = w & 3, wk = w >> 2;
  const int xcd = blockIdx.x & 7, bi = blockIdx.x >> 3, tm = xcd * 2 + (bi & 1), tn = bi >> 1;
  const unsigned lds0 = (unsigned)(uintptr_t)lds;
  const lds_cptr L = (lds_cptr)lds;
  const unsigned vlin = (unsigned)(lane * 16);
  u32x4 af[16][2];
  const char* ab = (const char*)ofm + (size_t)(wk * 16 * 128 + tm * 8 + wm * 2) * 1024;
#define AL(i, h) GLOAD4(af[i][h], ab + (size_t)((i) ^ tn) * 131072, vlin, (h) * 1024)
  const unsigned wvoff = (unsigned)(((w ^ ((tn & 1) << 2)) * 8 + (lane >> 3)) * 2048 + tn * 128) + wimg_src_chunk(lane);
#define OWL(q) glds16((const char*)wo + ((q) ^ (tn >> 1)) * 64 * 2048, wvoff, lds0 + ((q) * 8 + w) * 1024)
  AL(0, 0); AL(0, 1); OWL(0); OWL(1); OWL(8); OWL(9); AL(1, 0); AL(1, 1);
  const int g = lane >> 4, tok = tm * 128 + wm * 32 + wk * 16 + (lane & 15);
  const unsigned xvoff = (unsigned)((lane >> 3) * 2048 + (((lane & 7) ^ (lane >> 3)) << 4)), mvoff = (unsigned)(((lane >> 4) & 1) * 8192 + (lane & 15) * 16);
#define OEL(k) do { if ((k) < 2) glds16((const char*)x + (size_t)(tm * 128 + wm * 32 + wk * 16 + (k) * 8) * 2048 + tn * 128, xvoff, lds0 + 131072 + w * 4096 + (k) * 1024); \
    else glds16(modf + 2048 + tn * 64, mvoff, lds0 + 131072 + w * 4096 + 2048); } while (0)
  unsigned up[4]; const unsigned uvoff = (unsigned)(((blockIdx.x * 8 + w) * 7 + (lane < 7 ? lane : 0)) * 4);
#define UL(k) GLOAD1(up[k], utp + (k) * 14336, uvoff)
  f32x4 acc[2][4] = {};
  {
    lds_cptr a[4];
#pragma unroll
    for (int t = 0; t < 4; ++t) { a[t] = L + wk * 65536 + wimg_rd(lane, t); PIN(a[t]); PIN2(acc[0][t], acc[1][t]); }
    asm volatile("s_waitcnt vmcnt(2)\n\ts_barrier" ::: "memory");
    ST(1);
    bf16x8 wf[4], wn[4];
#pragma unroll
    for (int t = 0; t < 4; ++t) wf[t] = ldtr2(a[t], a[t] + 2048);
#define OMM(i, t) do { acc[0][t] = MFMA16(wf[t], __builtin_bit_cast(bf16x8, af[i][0]), acc[0][t]); acc[1][t] = MFMA16(wf[t], __builtin_bit_cast(bf16x8, af[i][1]), acc[1][t]); PIN2(acc[0][t], acc[1][t]); SBAR(); } while (0)
#define OSTEP(i, N, B, L0, L1, L2, L3) do { asm volatile("s_waitcnt vmcnt(" #N ")" : "+v"(af[i][0]), "+v"(af[i][1])); if (B) BAR(); \
    _Pragma("unroll") for (int t = 0; t < 4; ++t) wn[t] = (i) < 15 ? ldtr2(a[t] + ((i) + 1) * 4096, a[t] + ((i) + 1) * 4096 + 2048) : wf[t]; SBAR(); \
    OMM(i, 0); L0; OMM(i, 1); L1; OMM(i, 2); L2; OMM(i, 3); L3; _Pragma("unroll") for (int t = 0; t < 4; ++t) wf[t] = wn[t]; } while (0)
    OSTEP(0, 6, 0, AL(2, 0); AL(2, 1), OWL(2), OWL(3); OWL(10), OWL(11));
    OSTEP(1, 6, 0, AL(3, 0), AL(3, 1), AL(4, 0), AL(4, 1));
    OSTEP(2, 8, 0, OWL(4), AL(5, 0), AL(5, 1), OWL(5));
    OSTEP(3, 6, 1, OWL(12), AL(6, 0), AL(6, 1), (void)0);
    OSTEP(4, 7, 0, OWL(13), AL(7, 0), AL(7, 1), (void)0);
    OSTEP(5, 7, 0, OWL(6), AL(8, 0), AL(8, 1), (void)0);
    OSTEP(6, 6, 0, OWL(7), AL(9, 0), AL(9, 1), (void)0);
    OSTEP(7, 6, 1, OWL(14), AL(10, 0), AL(10, 1), (void)0);
    OSTEP(8, 6, 0, OWL(15), AL(11, 0), AL(11, 1), (void)0);
    OSTEP(9, 6, 0, OEL(0), AL(12, 0), AL(12, 1), (void)0);
    OSTEP(10, 6, 0, OEL(1), AL(13, 0), AL(13, 1), (void)0);
    OSTEP(11, 6, 1, OEL(2), AL(14, 0), AL(14, 1), (void)0);
    OSTEP(12, 6, 0, UL(0), AL(15, 0), AL(15, 1), (void)0);
    OSTEP(13, 6, 0, UL(1), UL(2), UL(3), (void)0);
    OSTEP(14, 6, 0, (void)0, (void)0, (void)0, (void)0);
    OSTEP(15, 3, 0, (void)0, (void)0, (void)0, (void)0);
  }
  ST(2);
  lds_ptr xb = (lds_ptr)lds + 131072;
  u32x2 xr[4]; f32x4 gav[4], cbv[4];
  asm volatile("s_waitcnt vmcnt(0)" : "+v"(up[0]), "+v"(up[1]), "+v"(up[2]), "+v"(up[3]) :: "memory");
  if (lane < 7) utf[uvoff >> 2] = (__uint_as_float(up[0]) + __uint_as_float(up[1])) + (__uint_as_float(up[2]) + __uint_as_float(up[3]));
#pragma unroll
  for (int t = 0; t < 4; ++t) { const lds_cptr e = xb + w * 4096;
    xr[t] = *(const LAS u32x2*)(e + (lane & 15) * 128 + (((2 * t + (g >> 1)) ^ (lane & 7)) << 4) + (g & 1) * 8);
    gav[t] = *(const LAS f32x4*)(e + 2048 + t * 64 + g * 16); cbv[t] = *(const LAS f32x4*)(e + 2048 + 256 + t * 64 + g * 16); }
  asm volatile("s_waitcnt lgkmcnt(0)" : "+v"(xr[0]), "+v"(xr[1]), "+v"(xr[2]), "+v"(xr[3]), "+v"(gav[0]), "+v"(gav[1]), "+v"(gav[2]), "+v"(gav[3]),
               "+v"(cbv[0]), "+v"(cbv[1]), "+v"(cbv[2]), "+v"(cbv[3]) :: "memory");
#pragma unroll
  for (int t = 0; t < 4; ++t) *(LAS f32x4*)(xb + w * 4096 + t * 1024 + lane * 16) = wk ? acc[0][t] : acc[1][t];
  asm volatile("s_waitcnt lgkmcnt(0)\n\ts_barrier" ::: "memory");
  float v[16]; float s = 0.f;
#pragma unroll
  for (int t = 0; t < 4; ++t) {
    const f32x4 o = *(const LAS f32x4*)(xb + (w ^ 4) * 4096 + t * 1024 + lane * 16) + (wk ? acc[1][t] : acc[0][t]), ga = gav[t];
    v[4 * t] = rbf(bflo(xr[t][0]) + ga[0] * o[0]); v[4 * t + 1] = rbf(bfhi(xr[t][0]) + ga[1] * o[1]);
    v[4 * t + 2] = rbf(bflo(xr[t][1]) + ga[2] * o[2]); v[4 * t + 3] = rbf(bfhi(xr[t][1]) + ga[3] * o[3]);
    s += (v[4 * t] + v[4 * t + 1]) + (v[4 * t + 2] + v[4 * t + 3]);
  }
  const size_t fo = ((size_t)(tn * 2 * 128 + (tok >> 4)) * 64 + lane) * 8;
#pragma unroll
  for (int jj = 0; jj < 2; ++jj) {
    const float* p = v + 8 * jj; const f32x4 c0 = cbv[2 * jj], c1 = cbv[2 * jj + 1];
    store16(yfm + fo + jj * 65536, (u32x4){cvtpk(p[0] * c0[0], p[1] * c0[1]), cvtpk(p[2] * c0[2], p[3] * c0[3]), cvtpk(p[4] * c1[0], p[5] * c1[1]), cvtpk(p[6] * c1[2], p[7] * c1[3])});
    store16(x1fm + fo + jj * 65536, (u32x4){cvtpk(p[0], p[1]), cvtpk(p[2], p[3]), cvtpk(p[4], p[5]), cvtpk(p[6], p[7])});
  }
  const float mw = sum4g(s) * (1.f / 64.f); float q = 0.f;
#pragma unroll
  for (int j = 0; j < 16; ++j) { const float d = v[j] - mw; q += d * d; }
  q = sum4g(q);
  if (g == 0) stat[((tok >> 4) * 16 + tn) * 16 + (lane & 15)] = (f32x2){mw, q};
  ST(6);
  ST_END(3);
}

__global__ __launch_bounds__(512, 2) void k5_gateup(const bf16_t* yfm, const bf16_t* wg, const bf16_t* wu, const f32x2* stat, const float* utp, bf16_t* actfm STAMP_ARG) {
  extern __shared__ __attribute__((aligned(16))) char lds[];
  ST_DECL; ST(0);
  KEEP_S(yfm); KEEP_S(wg); KEEP_S(wu); KEEP_S(stat); KEEP_S(utp); KEEP_S(actfm);
  ST(7);
  const int tid = threadIdx.x, lane = tid & 63, w = RFL(tid >> 6), wm = w & 3, wn = w >> 2;
  const int xcd = blockIdx.x & 7, bi = blockIdx.x >> 3, tm = (xcd & 1) * 4 + (bi & 3), tn = (xcd >> 1) * 7 + (bi >> 2);
  const unsigned lds0 = (unsigned)(uintptr_t)lds;
  const lds_cptr L = (lds_cptr)lds;
  const char* sA = (const char*)yfm + (size_t)(tm * 16 + 2 * w) * 1024;
  const char* sB = (const char*)(w < 4 ? wg : wu) + (size_t)(tn * 128 + ((w >> 1) & 1) * 64) * 2 + (size_t)(16 * (w & 1)) * 7168;
  const unsigned vA = (unsigned)(lane * 16), vB = (unsigned)((lane >> 3) * 7168) + wimg_src_chunk(lane);
#define K5_ISSUE(s, slot) do { const unsigned d_ = lds0 + (slot) * 32768 + w * 2048; \
    glds16(sA + (size_t)(s) * 131072, vA, d_); glds16(sA + (size_t)(s) * 131072 + 1024, vA, d_ + 1024); \
    glds16(sB + (size_t)(s) * 32 * 7168, vB, d_ + 16384); glds16(sB + (size_t)(s) * 32 * 7168 + 8 * 7168, vB, d_ + 16384 + 1024); } while (0)
  { const char* sS = (const char*)stat + tm * 32768 + w * 4096; const unsigned d_ = lds0 + 3 * 32768 + w * 4096;
    glds16(sS, vA, d_); glds16(sS + 1024, vA, d_ + 1024); glds16(sS + 2048, vA, d_ + 2048); glds16(sS + 3072, vA, d_ + 3072); }
  glds16((const char*)utp + tn * 2048 + (w & 1) * 1024, vA, lds0 + 131072 + w * 1024);
  K5_ISSUE(0, 0); K5_ISSUE(1, 1);
  lds_cptr ra[2], rb[2][4];
#pragma unroll
  for (int h = 0; h < 2; ++h) { ra[h] = L + h * 65536 + wm * 4096 + lane * 16; PIN(ra[h]);
#pragma unroll
    for (int t = 0; t < 4; ++t) { rb[h][t] = L + h * 65536 + 16384 + wn * 4096 + wimg_rd(lane, t); PIN(rb[h][t]); } }
  asm volatile("s_waitcnt vmcnt(8)\n\ts_barrier" ::: "memory");
  K5_ISSUE(2, 2);
  f32x4 ag[4][4], au[4][4]; float rs[4];
  {
    float sd[4], nmean[4];
#pragma unroll
    for (int m = 0; m < 4; ++m) {
      const lds_cptr sp = L + 3 * 32768 + (wm * 4 + m) * 2048 + (lane >> 4) * 512 + (lane & 15) * 8;
      f32x2 st[4];
#pragma unroll
      for (int j = 0; j < 4; ++j) st[j] = *(const LAS f32x2*)(sp + j * 128);
      const float mean = sum4g((st[0][0] + st[1][0]) + (st[2][0] + st[3][0])) * (1.f / 16.f); float q = 0.f;
#pragma unroll
      for (int j = 0; j < 4; ++j) { const float d = st[j][0] - mean; q += st[j][1] + 64.f * d * d; }
      const float var = sum4g(q) * (1.f / D_) + LN_EPS;
      rs[m] = __builtin_amdgcn_rsqf(var); sd[m] = var * rs[m]; nmean[m] = -mean;
    }
#pragma unroll
    for (int t = 0; t < 4; ++t) { const LAS float* p = (const LAS float*)(L + 131072) + wn * 64 + t * 16 + 4 * (lane >> 4);
      const f32x4 ug = *(const LAS f32x4*)(p), tg = *(const LAS f32x4*)(p + 128), uu = *(const LAS f32x4*)(p + 256), tu = *(const LAS f32x4*)(p + 384);
#pragma unroll
      for (int m = 0; m < 4; ++m) { ag[m][t] = tg * sd[m] + ug * nmean[m]; au[m][t] = tu * sd[m] + uu * nmean[m]; PIN2(ag[m][t], au[m][t]); } }
  }
  asm volatile("s_waitcnt vmcnt(8) lgkmcnt(0)\n\ts_barrier" ::: "memory");
  ST(1);
  if (wn) BAR();
#define K5_STEP(slot, sn, LAST) do { \
    bf16x8 xa[4], wgf[4], wuf[4]; \
    _Pragma("unroll") for (int t = 0; t < 4; ++t) { const lds_cptr p_ = rb[(slot) >> 1][t] + ((slot) & 1) * 32768; wgf[t] = ldtr2(p_, p_ + 2048); wuf[t] = ldtr2(p_ + 8192, p_ + 8192 + 2048); } \
    _Pragma("unroll") for (int m = 0; m < 4; ++m) xa[m] = ld128(ra[(slot) >> 1] + ((slot) & 1) * 32768 + m * 1024); \
    if ((sn) < 32) { K5_ISSUE(sn, ((slot) + 3) & 3); WAITV(8); } else WAITV(0); \
    asm volatile("s_waitcnt lgkmcnt(0)\n\ts_barrier" ::: "memory"); SBAR(); \
    __builtin_amdgcn_s_setprio(1); \
    _Pragma("unroll") for (int m = 0; m < 4; ++m) _Pragma("unroll") for (int t = 0; t < 4; ++t) { ag[m][t] = MFMA16(wgf[t], xa[m], ag[m][t]); au[m][t] = MFMA16(wuf[t], xa[m], au[m][t]); } \
    __builtin_amdgcn_s_setprio(0); SBAR(); \
    if (!(LAST)) BAR(); } while (0)
#pragma unroll 1
  for (int s = 0; s < 32; s += 4) {
    K5_STEP(0, s + 3, false);
    K5_STEP(1, s + 4, false);
    K5_STEP(2, s + 5, false);
    K5_STEP(3, s + 6, s == 28 && wn);
  }
  ST(2);
#pragma unroll
  for (int m = 0; m < 4; ++m) {
    const float r2 = rs[m] * rs[m], k0 = -2.302208198f * rs[m], k1 = (0.044715f * k0) * r2, iv = __builtin_amdgcn_rcpf(r2);
#pragma unroll
    for (int jj = 0; jj < 2; ++jj) {
      u32x4 o;
#pragma unroll
      for (int e = 0; e < 4; ++e) {
        const f32x4 a4 = ag[m][2 * jj + (e >> 1)], u4 = au[m][2 * jj + (e >> 1)];
        const f32x2 a = {a4[2 * (e & 1)], a4[2 * (e & 1) + 1]}, u = {u4[2 * (e & 1)], u4[2 * (e & 1) + 1]};
        const f32x2 z = a * ((a * a) * k1 + k0);
        const f32x2 ex = {__builtin_amdgcn_exp2f(z[0]), __builtin_amdgcn_exp2f(z[1])};
        const f32x2 dn = ex * iv + iv;
        const f32x2 rc = {__builtin_amdgcn_rcpf(dn[0]), __builtin_amdgcn_rcpf(dn[1])};
        const f32x2 r = (a * u) * rc;
        o[e] = cvtpk(r[0], r[1]);
      }
      store16(actfm + ((size_t)((tn * 4 + wn * 2 + jj) * 128 + tm * 16 + wm * 4 + m) * 64 + lane) * 8, o);
      SBAR();
    }
  }
  ST(6);
  ST_END(3);
}

#define K6_NI 2
#define K6ROT 0
__global__ __launch_bounds__(512, 2) void k6_down(const bf16_t* actfm, const bf16_t* wd, const bf16_t* x1fm, const float* modf, bf16_t* out STAMP_ARG) {
  extern __shared__ __attribute__((aligned(16))) char lds[];
  ST_DECL; ST(0);
  KEEP_S(actfm); KEEP_S(wd); KEEP_S(x1fm); KEEP_S(modf); KEEP_S(out);
  ST(7);
  const int tid = threadIdx.x, lane = tid & 63, w = RFL(tid >> 6), wm = w & 3, wk = w >> 2;
  const int xcd = blockIdx.x & 7, bi = blockIdx.x >> 3, tm = (xcd >> 1) * 4 + (bi & 3), tn = (xcd & 1) * 8 + (bi >> 2);
  const unsigned lds0 = (unsigned)(uintptr_t)lds;
  const lds_cptr L = (lds_cptr)lds;
  const char* sW = (const char*)wd + (size_t)(16 * w) * 2048 + tn * 128;
  const unsigned vW = (unsigned)((lane >> 3) * 2048) + wimg_src_chunk(lane);
  const char* sA = (const char*)actfm + ((size_t)(2 * wk) * 128 + tm * 8 + wm * 2) * 1024;
  const unsigned vA = (unsigned)(lane * 16);
  u32x4 ar[4][4];
  int win = 0, pos = K6ROT == 1 ? 2 * (bi & 3) : 0, sl = K6ROT ? (pos + (bi >> 2) + 2 * (bi & 3)) % 7 : 0;
  const char* w_; const char* a_; unsigned d_;
#define K6_NEXT(slot) do { const int st_ = win + sl; d_ = lds0 + (slot) * 16384 + w * 2048; w_ = sW + (size_t)st_ * 128 * 2048; a_ = sA + (size_t)st_ * 524288; \
    sl = sl == 6 ? 0 : sl + 1; if (pos == 6) { pos = 0; win = win == 21 ? 0 : win + 7; } else ++pos; } while (0)
#define K6_LD(u, k) do { if ((k) == 0) glds16(w_, vW, d_); else if ((k) == 1) glds16(w_ + 8 * 2048, vW, d_ + 1024); \
    else if ((k) == 2) GLOAD4(ar[u][0], a_, vA, 0); else if ((k) == 3) GLOAD4(ar[u][1], a_, vA, 1024); \
    else if ((k) == 4) GLOAD4(ar[u][2], a_ + 131072, vA, 0); else GLOAD4(ar[u][3], a_ + 131072, vA, 1024); } while (0)
#define K6_ISSUE(u, slot) do { K6_NEXT(slot); K6_LD(u, 0); K6_LD(u, 1); K6_LD(u, 2); K6_LD(u, 3); K6_LD(u, 4); K6_LD(u, 5); } while (0)
  K6_ISSUE(0, 0); K6_ISSUE(1, 1); K6_ISSUE(2, 2);
  f32x4 acc[2][4] = {};
  lds_cptr a[4];
#pragma unroll
  for (int t = 0; t < 4; ++t) a[t] = L + wk * 8192 + wimg_rd(lane, t);
  const int g = lane >> 4, tok = tm * 128 + wm * 32 + wk * 16 + (lane & 15), col0 = tn * 64 + 4 * g;
  u32x4 xr[2], gbv[4];
  const char* x_ = (const char*)x1fm + (size_t)(tn * 2 * 128 + tm * 8 + wm * 2 + wk) * 1024; const unsigned mo_ = (unsigned)(col0 * 4);
#define K6_EL(k) do { if ((k) == 0) GLOAD4(xr[0], x_, vA, 0); else if ((k) == 1) GLOAD4(xr[1], x_ + 131072, vA, 0); else GLOAD4(gbv[(k) - 2], modf + 5120, mo_, 64 * ((k) - 2)); } while (0)
#define K6_MM(u, i, t) do { acc[0][t] = MFMA16(wf[i][t], __builtin_bit_cast(bf16x8, ar[u][2 * (i)]), acc[0][t]); acc[1][t] = MFMA16(wf[i][t], __builtin_bit_cast(bf16x8, ar[u][2 * (i) + 1]), acc[1][t]); \
    PIN2(acc[0][t], acc[1][t]); SBAR(); } while (0)
#define K6_L(u, MODE, k) do { if ((MODE) == 1) K6_LD(((u) + 3) & 3, k); else if ((MODE) == 2) K6_EL(k); SBAR(); } while (0)
#define K6_STEP(u, WN, MODE) do { \
    asm volatile("s_waitcnt vmcnt(" #WN ")\n\ts_barrier" : "+v"(ar[u][0]), "+v"(ar[u][1]), "+v"(ar[u][2]), "+v"(ar[u][3]) :: "memory"); \
    bf16x8 wf[2][4]; \
    _Pragma("unroll") for (int i = 0; i < 2; ++i) _Pragma("unroll") for (int t = 0; t < 4; ++t) wf[i][t] = ldtr2(a[t] + (u) * 16384 + i * 4096, a[t] + (u) * 16384 + i * 4096 + 2048); \
    if ((MODE) == 1) K6_NEXT(((u) + 3) & 3); SBAR(); \
    K6_L(u, MODE, 0); K6_MM(u, 0, 0); K6_L(u, MODE, 1); K6_MM(u, 0, 1); K6_MM(u, 0, 2); K6_L(u, MODE, 2); K6_MM(u, 0, 3); K6_L(u, MODE, 3); \
    K6_MM(u, 1, 0); K6_MM(u, 1, 1); K6_L(u, MODE, 4); K6_MM(u, 1, 2); K6_L(u, MODE, 5); K6_MM(u, 1, 3); } while (0)
  for (int s = 0; s < 24; s += 4) { K6_STEP(0, 12, 1); K6_STEP(1, 12, 1); K6_STEP(2, 12, 1); K6_STEP(3, 12, 1); }
  K6_STEP(0, 12, 1); K6_STEP(1, 12, 2); K6_STEP(2, 12, 0); K6_STEP(3, 6, 0);
  ST(2);
  lds_ptr xb = (lds_ptr)lds + 131072;
#pragma unroll
  for (int t = 0; t < 4; ++t) *(LAS f32x4*)(xb + w * 4096 + t * 1024 + lane * 16) = wk ? acc[0][t] : acc[1][t];
  asm volatile("s_waitcnt vmcnt(0) lgkmcnt(0)\n\ts_barrier" : "+v"(xr[0]), "+v"(xr[1]), "+v"(gbv[0]), "+v"(gbv[1]), "+v"(gbv[2]), "+v"(gbv[3]) :: "memory");
#pragma unroll
  for (int jj = 0; jj < 2; ++jj) {
    unsigned pk[2][2];
#pragma unroll
    for (int tt = 0; tt < 2; ++tt) { const int t = 2 * jj + tt;
      const f32x4 o = *(const LAS f32x4*)(xb + (w ^ 4) * 4096 + t * 1024 + lane * 16) + (wk ? acc[1][t] : acc[0][t]), gb = __builtin_bit_cast(f32x4, gbv[t]);
      pk[tt][0] = cvtpk(bflo(xr[jj][2 * tt]) + gb[0] * o[0], bfhi(xr[jj][2 * tt]) + gb[1] * o[1]);
      pk[tt][1] = cvtpk(bflo(xr[jj][2 * tt + 1]) + gb[2] * o[2], bfhi(xr[jj][2 * tt + 1]) + gb[3] * o[3]); }
    auto r0 = __builtin_amdgcn_permlane16_swap(pk[0][0], pk[1][0], false, false);
    auto r1 = __builtin_amdgcn_permlane16_swap(pk[0][1], pk[1][1], false, false);
    *(u32x4*)(out + (size_t)tok * D_ + tn * 64 + jj * 32 + (g & 1) * 16 + (g >> 1) * 8) = (u32x4){r0[0], r1[0], r0[1], r1[1]};
  }
  ST(6);
  ST_END(3);
}

extern "C" void kernel_launch(void* const* d_in, const int* in_sizes, int n_in, void* d_out, int out_size, void* d_ws, size_t ws_size, hipStream_t stream) {
  const bf16_t* x = (const bf16_t*)d_in[0]; const bf16_t* wq = (const bf16_t*)d_in[1]; const bf16_t* wk = (const bf16_t*)d_in[2];
  const bf16_t* wv = (const bf16_t*)d_in[3]; const bf16_t* wo = (const bf16_t*)d_in[4]; const bf16_t* wg = (const bf16_t*)d_in[6];
  const bf16_t* wu = (const bf16_t*)d_in[7]; const bf16_t* wd = (const bf16_t*)d_in[8]; const bf16_t* c = (const bf16_t*)d_in[9];
  const bf16_t* wmod = (const bf16_t*)d_in[10];
  char* ws = (char*)d_ws;
  float* modp = (float*)(ws + WS_MODP); float* modf = (float*)(ws + WS_MODF); bf16_t* cvec = (bf16_t*)(ws + WS_CVEC);
  bf16_t* wkvf = (bf16_t*)(ws + WS_WKVF); bf16_t* kvf = (bf16_t*)(ws + WS_KVF);
  float* utp = (float*)(ws + WS_UTP); float* utf = (float*)(ws + WS_UTF); f32x2* stat = (f32x2*)(ws + WS_STAT);
  bf16_t* hfm = (bf16_t*)(ws + WS_H); bf16_t* ofm = (bf16_t*)(ws + WS_O); bf16_t* x1fm = (bf16_t*)(ws + WS_X1); bf16_t* yfm = (bf16_t*)(ws + WS_Y); bf16_t* actfm = (bf16_t*)(ws + WS_ACT);
  (void)hipFuncSetAttribute((const void*)k3_attn, hipFuncAttributeMaxDynamicSharedMemorySize, 163840);
  (void)hipFuncSetAttribute((const void*)k4_oproj, hipFuncAttributeMaxDynamicSharedMemorySize, 163840);
  (void)hipFuncSetAttribute((const void*)k5_gateup, hipFuncAttributeMaxDynamicSharedMemorySize, 139264);
  (void)hipFuncSetAttribute((const void*)k6_down, hipFuncAttributeMaxDynamicSharedMemorySize, 163840);
  k1_mod<<<288, 256, 0, stream>>>(c, wmod, wk, wv, modp, wkvf, x STAMP_PASS(0));
  k2_ln_kv<<<256, 512, 0, stream>>>(x, modp, wkvf, wq, hfm, kvf, modf, cvec, c, wmod STAMP_PASS(1));
  k3_attn<<<256, 512, 163840, stream>>>(hfm, wq, kvf, ofm, wg, wu, cvec, utp, wo, wd, x, modp, modf STAMP_PASS(2));
  k4_oproj<<<256, 512, 163840, stream>>>(ofm, wo, x, modf, x1fm, yfm, stat, utp, utf STAMP_PASS(3));
  k5_gateup<<<224, 512, 139264, stream>>>(yfm, wg, wu, stat, utf, actfm STAMP_PASS(4));
  k6_down<<<256, 512, 163840, stream>>>(actfm, wd, x1fm, modf, (bf16_t*)d_out STAMP_PASS(5));
}
```

```cpp
#include <hip/hip_runtime.h>
#include <stdint.h>

typedef unsigned short bf16_t;
typedef short bf16x8 __attribute__((ext_vector_type(8)));
typedef short s16x4 __attribute__((ext_vector_type(4)));
typedef float f32x2 __attribute__((ext_vector_type(2)));
typedef float f32x4 __attribute__((ext_vector_type(4)));
typedef float f32x16 __attribute__((ext_vector_type(16)));
typedef unsigned u32x2 __attribute__((ext_vector_type(2)));
typedef unsigned u32x4 __attribute__((ext_vector_type(4)));
typedef __bf16 bf16x2_t __attribute__((ext_vector_type(2)));
#define LAS __attribute__((address_space(3)))
typedef LAS const char* lds_cptr;
typedef LAS char* lds_ptr;
#define DI __device__ __forceinline__

constexpr int S_ = 2048, D_ = 1024, F_ = 3584, NMOD = 6144;
constexpr float LN_EPS = 1e-5f;
constexpr float QSCALE = 0.17677669529663687f * 1.4426950408889634f;

constexpr size_t WS_MODP = 0;
constexpr size_t WS_MODF = 128 * 1024;
constexpr size_t WS_CVEC = 160 * 1024;
constexpr size_t WS_WKVF = 192 * 1024;
constexpr size_t WS_KVF  = 384 * 1024;
constexpr size_t WS_UTF  = 640 * 1024;
constexpr size_t WS_UTP  = 32u << 20;
constexpr size_t WS_FLAG = 33u << 20;
#define PARTOFF 1
constexpr size_t WS_PART = (size_t)PARTOFF << 20;
constexpr size_t WS_STAT = 768 * 1024;
constexpr size_t WS_H    = 1u << 20;
constexpr size_t WS_O    = 5u << 20;
constexpr size_t WS_X1   = 9u << 20;
constexpr size_t WS_Y    = 13u << 20;
constexpr size_t WS_ACT  = 17u << 20;

DI unsigned cvtpk(float lo, float hi) { f32x2 v = {lo, hi}; bf16x2_t b = __builtin_convertvector(v, bf16x2_t); return __builtin_bit_cast(unsigned, b); }
DI float bflo(unsigned u) { return __uint_as_float(u << 16); }
DI float bfhi(unsigned u) { return __uint_as_float(u & 0xffff0000u); }
DI float rbf(float x) { return bflo(cvtpk(x, 0.f) & 0xffffu); }
DI float bf2f(bf16_t b) { return __uint_as_float((unsigned)b << 16); }
DI void glds16(const void* sbase, unsigned voff, unsigned lds_dst) {
  unsigned keep;
  asm volatile("s_mov_b32 %0, m0\n\ts_mov_b32 m0, %3\n\ts_nop 0\n\tglobal_load_lds_dwordx4 %1, %2\n\ts_mov_b32 m0, %0"
               : "=&s"(keep) : "v"(voff), "s"(sbase), "s"(lds_dst) : "memory");
}
#define GLOAD4(dst, sbase, voff, imm) asm volatile("global_load_dwordx4 %0, %1, %2 offset:%3" : "=v"(dst) : "v"(voff), "s"(sbase), "n"(imm) : "memory")
#define GLOAD4NT(dst, sbase, voff) asm volatile("global_load_dwordx4 %0, %1, %2 nt" : "=v"(dst) : "v"(voff), "s"(sbase) : "memory")
#define GLOAD1(dst, sbase, voff) asm volatile("global_load_dword %0, %1, %2" : "=v"(dst) : "v"(voff), "s"(sbase) : "memory")
#define GLOADH(dst, sbase, voff) asm volatile("global_load_ushort %0, %1, %2" : "=v"(dst) : "v"(voff), "s"(sbase) : "memory")
typedef short v4i16_t __attribute__((ext_vector_type(4)));
DI s16x4 ldtr(lds_cptr p) { return __builtin_bit_cast(s16x4, __builtin_amdgcn_ds_read_tr16_b64_v4i16((LAS v4i16_t*)p)); }
DI bf16x8 ldtr2(lds_cptr p0, lds_cptr p1) { s16x4 a = ldtr(p0), b = ldtr(p1); return (bf16x8){a[0], a[1], a[2], a[3], b[0], b[1], b[2], b[3]}; }
DI bf16x8 ld128(lds_cptr p) { return *(const LAS bf16x8*)p; }
#define MFMA32(a, b, c) __builtin_amdgcn_mfma_f32_32x32x16_bf16(a, b, c, 0, 0, 0)
#define MFMA16(a, b, c) __builtin_amdgcn_mfma_f32_16x16x32_bf16(a, b, c, 0, 0, 0)
#define SBAR() __builtin_amdgcn_sched_barrier(0)
#define BAR() asm volatile("s_barrier" ::: "memory")
#define WAITV(n) asm volatile("s_waitcnt vmcnt(" #n ")" ::: "memory")
#define WAITL() asm volatile("s_waitcnt lgkmcnt(0)" ::: "memory")
#define RFL(x) __builtin_amdgcn_readfirstlane(x)
#define PIN(x) asm volatile("" : "+v"(x))
#define PIN2(x, y) asm volatile("" : "+v"(x), "+v"(y))
#define WT 1
DI void store16(void* p, u32x4 v) {
  asm volatile("global_store_dwordx4 %0, %1, off sc1\n\ts_nop 1" :: "v"(p), "v"(v) : "memory");
}
DI float sum4g(float x) {
  auto a = __builtin_amdgcn_permlane16_swap(__float_as_uint(x), __float_as_uint(x), false, false); x = __uint_as_float(a[0]) + __uint_as_float(a[1]);
  auto b = __builtin_amdgcn_permlane32_swap(__float_as_uint(x), __float_as_uint(x), false, false); return __uint_as_float(b[0]) + __uint_as_float(b[1]);
}
template <int CTRL> DI float dpp_add(float x) { return x + __int_as_float(__builtin_amdgcn_update_dpp(0, __float_as_int(x), CTRL, 0xf, 0xf, true)); }
DI float sum64(float x) { x = dpp_add<0xB1>(x); x = dpp_add<0x4E>(x); x = dpp_add<0x141>(x); x = dpp_add<0x140>(x); return sum4g(x); }
#define KEEP_S(p) asm volatile("" :: "s"(p))
#define TOUCH(dummy, sbase, voff) asm volatile("global_load_dword %0, %1, %2" : "+v"(dummy) : "v"(voff), "s"(sbase) : "memory")
#define TOUCH255(sbase, voff) asm volatile("global_load_dword v255, %0, %1" :: "v"(voff), "s"(sbase) : "memory", "v255")
#define CODEPF 1
#define SYM_ADDR(dst, name) asm volatile("s_getpc_b64 vcc\n\ts_add_u32 vcc_lo, vcc_lo, " #name "@rel32@lo+4\n\ts_addc_u32 vcc_hi, vcc_hi, " #name "@rel32@hi+12\n\ts_mov_b64 %0, vcc" : "=s"(dst) :: "vcc", "scc")
#define CODE_OFF(a, b, i, lane) ({ const int len_ = (int)((b) - (a)) - 4, o_ = (i) * 8192 + (lane) * 128; (unsigned)(o_ < len_ ? o_ : (len_ > 0 ? len_ : 0)); })
typedef __bf16 bf16x8n __attribute__((ext_vector_type(8)));
typedef __bf16 bf16x16n __attribute__((ext_vector_type(16)));
typedef unsigned u32x8 __attribute__((ext_vector_type(8)));

#define STAMP_ARG
#define STAMP_PASS(k)
#define ST_DECL
#define ST(i)
#define ST_END(i)

#define XTOUCH 1
DI float silu_bf(bf16_t c) { const float cv = bf2f(c); return rbf(cv * __builtin_amdgcn_rcpf(1.f + __expf(-cv))); }
extern "C" __global__ __launch_bounds__(256, 2) void k1_mod(const bf16_t* __restrict__ c, const bf16_t* __restrict__ wmod, const bf16_t* __restrict__ wk,
                                                 const bf16_t* __restrict__ wv, float* __restrict__ modp, bf16_t* __restrict__ wkvf, const bf16_t* __restrict__ x STAMP_ARG) {
  __shared__ float red[4][64];
  ST_DECL; ST(0);
  KEEP_S(c); KEEP_S(wmod); KEEP_S(wk); KEEP_S(wv); KEEP_S(modp); KEEP_S(wkvf); KEEP_S(x);
  const int b = blockIdx.x, tid = threadIdx.x;
  ST(1);
  if (__builtin_expect(b < 256, 1)) {
    const int cg = b & 63, ks = b >> 6, l8 = tid & 7, r = tid >> 3;
    const bf16_t* wp = wmod + (size_t)(ks * 256 + r) * NMOD + cg * 64 + (cg >= 32 ? 1024 : 0) + l8 * 8;
    u32x4 w[8]; bf16_t cr[8];
#pragma unroll
    for (int i = 0; i < 8; ++i) w[i] = __builtin_nontemporal_load((const u32x4*)(wp + (size_t)i * 32 * NMOD));
#pragma unroll
    for (int i = 0; i < 8; ++i) cr[i] = c[ks * 256 + i * 32 + r];
    SBAR();
    unsigned dummy = 0;
    if (XTOUCH) TOUCH(dummy, x, (unsigned)((b & 7) * 524288 + ((b >> 3) * 128 + (tid >> 6) * 32 + (tid & 31)) * 128));
    ST(3);
    float acc[8] = {0, 0, 0, 0, 0, 0, 0, 0};
#pragma unroll
    for (int i = 0; i < 8; ++i) {
      const float sv = silu_bf(cr[i]);
#pragma unroll
      for (int j = 0; j < 4; ++j) { acc[2 * j] += sv * bflo(w[i][j]); acc[2 * j + 1] += sv * bfhi(w[i][j]); }
    }
#pragma unroll
    for (int j = 0; j < 8; ++j) acc[j] = sum4g(dpp_add<0x128>(acc[j]));
    if ((tid & 63) < 8) { *(f32x4*)&red[tid >> 6][l8 * 8] = (f32x4){acc[0], acc[1], acc[2], acc[3]}; *(f32x4*)&red[tid >> 6][l8 * 8 + 4] = (f32x4){acc[4], acc[5], acc[6], acc[7]}; }
    __syncthreads();
    ST(6);
    if (tid < 64) modp[ks * 4096 + cg * 64 + tid] = (red[0][tid] + red[1][tid]) + (red[2][tid] + red[3][tid]);
    asm volatile("s_waitcnt vmcnt(0)" : "+v"(dummy));
  } else {
    const int it = (b - 256) * 256 + tid, lane = it & 63, ct = (it >> 6) & 1, J = (it >> 7) & 31, z = it >> 12;
    unsigned dummy = 0;
    if (CODEPF && b < 272) { const char *c0, *c1; SYM_ADDR(c0, k2_ln_kv); SYM_ADDR(c1, k7_end); TOUCH(dummy, c0, CODE_OFF(c0, c1, ((b - 256) >> 3) * 4 + (tid >> 6), lane)); }
    const bf16_t* src = (z ? wv : wk) + (size_t)(32 * J + 8 * (lane >> 4)) * 32 + ct * 16 + (lane & 15);
    unsigned o[4];
#pragma unroll
    for (int j = 0; j < 4; ++j) o[j] = (unsigned)src[(2 * j) * 32] | ((unsigned)src[(2 * j + 1) * 32] << 16);
    *(u32x4*)(wkvf + (size_t)it * 8) = (u32x4){o[0], o[1], o[2], o[3]};
    asm volatile("s_waitcnt vmcnt(0)" : "+v"(dummy));
  }
  ST_END(7);
}

extern "C" __global__ __launch_bounds__(512, 2) void k2_ln_kv(const bf16_t* __restrict__ x, float* __restrict__ modp, const bf16_t* __restrict__ wkvf,
                                                   const bf16_t* __restrict__ wq, bf16_t* __restrict__ hfm, bf16_t* __restrict__ kvf,
                                                   float* __restrict__ modf, bf16_t* __restrict__ cvec, const bf16_t* __restrict__ c, const bf16_t* __restrict__ wmod STAMP_ARG) {
  __shared__ __attribute__((aligned(16))) char smem[34816 + 16384 + 8192 + 2048];
  ST_DECL; ST(0);
  KEEP_S(x); KEEP_S(modp); KEEP_S(wkvf); KEEP_S(wq); KEEP_S(hfm); KEEP_S(kvf); KEEP_S(modf); KEEP_S(cvec); KEEP_S(c); KEEP_S(wmod);
  ST(7);
  const int tid = threadIdx.x, lane = tid & 63, w = RFL(tid >> 6);
  const int xcd = blockIdx.x & 7, bi = blockIdx.x >> 3, z = bi & 1, tt = xcd * 16 + (bi >> 1);
  u32x4 xv[2][2];
#pragma unroll
  for (int rr = 0; rr < 2; ++rr)
#pragma unroll
    for (int hh = 0; hh < 2; ++hh) xv[rr][hh] = *(const u32x4*)(x + (size_t)(tt * 16 + 2 * w + rr) * D_ + hh * 512 + lane * 8);
  f32x4 mp[4];
#pragma unroll
  for (int p = 0; p < 4; ++p) mp[p] = *(const f32x4*)(modp + p * 4096 + tid * 4);
  SBAR();
  float* mt = (float*)(smem + 34816 + 16384);
  float v[2][16], rstd[2];
#pragma unroll
  for (int rr = 0; rr < 2; ++rr)
#pragma unroll
    for (int hh = 0; hh < 2; ++hh)
#pragma unroll
      for (int j = 0; j < 4; ++j) { v[rr][hh * 8 + 2 * j] = bflo(xv[rr][hh][j]); v[rr][hh * 8 + 2 * j + 1] = bfhi(xv[rr][hh][j]); }
  u32x4 wf[8];
  const char* wfp = (const char*)wkvf + (z * 64 + w * 8) * 1024; const unsigned vlin = (unsigned)(lane * 16);
  { f32x4 a = (mp[0] + mp[1]) + (mp[2] + mp[3]);
#pragma unroll
    for (int j = 0; j < 4; ++j) { a[j] = rbf(a[j]); if (w >= 4) a[j] = rbf(1.f + a[j]); }
    *(f32x4*)(mt + tid * 4) = a; }
  { float s0 = 0.f, s1 = 0.f;
#pragma unroll
    for (int j = 0; j < 16; ++j) { s0 += v[0][j]; s1 += v[1][j]; }
    PIN2(s0, s1); SBAR();
#pragma unroll
    for (int i = 0; i < 4; ++i) GLOAD4(wf[i], wfp, vlin, i * 1024);
    SBAR();
    s0 = sum64(s0) * (1.f / D_); s1 = sum64(s1) * (1.f / D_);
    float q0 = 0.f, q1 = 0.f;
#pragma unroll
    for (int j = 0; j < 16; ++j) { v[0][j] -= s0; q0 += v[0][j] * v[0][j]; v[1][j] -= s1; q1 += v[1][j] * v[1][j]; }
    rstd[0] = __builtin_amdgcn_rsqf(sum64(q0) * (1.f / D_) + LN_EPS); rstd[1] = __builtin_amdgcn_rsqf(sum64(q1) * (1.f / D_) + LN_EPS); }
  PIN2(rstd[0], rstd[1]);
  asm volatile("s_waitcnt lgkmcnt(0)\n\ts_barrier" ::: "memory");
  ST(1);
#pragma unroll
  for (int i = 4; i < 8; ++i) GLOAD4(wf[i], wfp + 4096, vlin, (i - 4) * 1024);
  const int gcg = blockIdx.x & 31, gks = blockIdx.x >> 5;
  u32x4 gw[2]; unsigned gc[2], mq[4], dummy;
#pragma unroll
  for (int i = 0; i < 2; ++i) { GLOAD4NT(gw[i], wmod + (size_t)(gks * 128 + i * 64) * NMOD + (gcg < 16 ? 2048 : 4096) + gcg * 64, (unsigned)((tid >> 3) * (NMOD * 2) + (tid & 7) * 16));
    GLOADH(gc[i], c + gks * 128 + i * 64, (unsigned)((tid >> 3) * 2)); }
  const int ce = blockIdx.x * 8 + (lane & 7);
  const int mi = (lane & 8) ? (((ce >> 3) & 1) ? 2048 : 3072) + 32 * (ce >> 6) + 16 * ((ce & 7) >> 2) + 4 * ((ce >> 4) & 3) + (ce & 3) : 3072 + blockIdx.x * 4 + (lane & 3);
#pragma unroll
  for (int p = 0; p < 4; ++p) GLOAD1(mq[p], modp + p * 4096, (unsigned)(mi * 4));
  { const int ln = bi * 256 + w * 32 + (lane & 31); GLOAD1(dummy, wq, (unsigned)((ln >> 3) * 2048 + (xcd & 1) * 1024 + (ln & 7) * 128)); }
  SBAR();
#pragma unroll
  for (int hh = 0; hh < 2; ++hh) {
    const f32x4 s0 = *(const f32x4*)(mt + hh * 512 + lane * 8), s1 = *(const f32x4*)(mt + hh * 512 + lane * 8 + 4);
    const f32x4 c0 = *(const f32x4*)(mt + 1024 + hh * 512 + lane * 8), c1 = *(const f32x4*)(mt + 1024 + hh * 512 + lane * 8 + 4);
#pragma unroll
    for (int rr = 0; rr < 2; ++rr) {
      const float* p = &v[rr][hh * 8]; const float r = rstd[rr];
      const u32x4 o = {cvtpk(p[0] * r * c0[0] + s0[0], p[1] * r * c0[1] + s0[1]), cvtpk(p[2] * r * c0[2] + s0[2], p[3] * r * c0[3] + s0[3]),
                       cvtpk(p[4] * r * c1[0] + s1[0], p[5] * r * c1[1] + s1[1]), cvtpk(p[6] * r * c1[2] + s1[2], p[7] * r * c1[3] + s1[3])};
      *(u32x4*)(smem + (hh * 64 + lane) * 272 + (2 * w + rr) * 16) = o;
    }
  }
  asm volatile("s_waitcnt vmcnt(9) lgkmcnt(0)\n\ts_barrier" : "+v"(wf[0]), "+v"(wf[1]), "+v"(wf[2]), "+v"(wf[3]), "+v"(wf[4]), "+v"(wf[5]), "+v"(wf[6]), "+v"(wf[7]) :: "memory");
  ST(2);
  f32x4 acc[2] = {};
#pragma unroll
  for (int jj = 0; jj < 4; ++jj) {
    const bf16x8 a = *(const bf16x8*)(smem + ((w * 4 + jj) * 4 + (lane >> 4)) * 272 + (lane & 15) * 16);
    acc[0] = MFMA16(a, __builtin_bit_cast(bf16x8, wf[jj * 2]), acc[0]); acc[1] = MFMA16(a, __builtin_bit_cast(bf16x8, wf[jj * 2 + 1]), acc[1]);
  }
  float* red = (float*)(smem + 34816);
  *(f32x4*)(red + ((w * 2) * 64 + lane) * 4) = acc[0]; *(f32x4*)(red + ((w * 2 + 1) * 64 + lane) * 4) = acc[1];
#pragma unroll
  for (int i = 0; i < 2; ++i) { const int it = i * 512 + tid, tok = it & 15, ch = z * 64 + (it >> 4);
    store16(hfm + ((size_t)((ch >> 1) * 64 + (tt >> 1)) * 64 + (tt & 1) * 16 + tok + 32 * (ch & 1)) * 8, *(const u32x4*)(smem + ch * 272 + tok * 16)); }
  asm volatile("s_waitcnt lgkmcnt(0)\n\ts_barrier" ::: "memory");
  ST(3);
  {
    float s = 0.f;
#pragma unroll
    for (int ww = 0; ww < 8; ++ww) s += red[ww * 512 + tid];
    const int r = tid & 3, ln = (tid >> 2) & 63, ct = tid >> 8;
    const int key = tt * 16 + 4 * (ln >> 4) + r, d = ct * 16 + (ln & 15), kt = key >> 5, kl = key & 31;
    int idx;
    if (z == 0) { const int dd = d & 15; idx = ((d >> 4) * 64 + kl + 32 * ((dd >> 2) & 1)) * 8 + (((dd >> 3) << 2) | (dd & 3)); }
    else { const int kk = kl & 15, rho = (d & 3) | (((d >> 3) & 1) << 2) | (((d >> 4) & 1) << 3) | (((d >> 2) & 1) << 4);
           idx = ((2 + (kl >> 4)) * 64 + rho + 32 * ((kk >> 2) & 1)) * 8 + (((kk >> 3) << 2) | (kk & 3)); }
    kvf[(size_t)kt * 2048 + idx] = (bf16_t)(cvtpk(z ? s : s * QSCALE, 0.f) & 0xffffu);
    asm volatile("s_waitcnt vmcnt(4)" : "+v"(gw[0]), "+v"(gw[1]), "+v"(gc[0]), "+v"(gc[1]), "+v"(mq[0]), "+v"(mq[1]), "+v"(mq[2]), "+v"(mq[3]) :: "memory");
    float ga[8] = {0, 0, 0, 0, 0, 0, 0, 0};
#pragma unroll
    for (int i = 0; i < 2; ++i) { const float sv = silu_bf((bf16_t)gc[i]);
#pragma unroll
      for (int j = 0; j < 4; ++j) { ga[2 * j] += sv * bflo(gw[i][j]); ga[2 * j + 1] += sv * bfhi(gw[i][j]); } }
    float mv = rbf((__uint_as_float(mq[0]) + __uint_as_float(mq[1])) + (__uint_as_float(mq[2]) + __uint_as_float(mq[3]))); if (mi >= 3072) mv = rbf(1.f + mv);
    if (w == 7) { if (lane < 4) modf[1024 + mi] = mv; else if (lane >= 8 && lane < 16) cvec[ce] = (bf16_t)(__float_as_uint(mv) >> 16); }
#pragma unroll
    for (int j = 0; j < 8; ++j) ga[j] = sum4g(dpp_add<0x128>(ga[j]));
    float* gr = (float*)(smem + 34816 + 16384 + 8192);
    if (lane < 8) { *(f32x4*)(gr + w * 64 + lane * 8) = (f32x4){ga[0], ga[1], ga[2], ga[3]}; *(f32x4*)(gr + w * 64 + lane * 8 + 4) = (f32x4){ga[4], ga[5], ga[6], ga[7]}; }
    asm volatile("s_waitcnt lgkmcnt(0)\n\ts_barrier" ::: "memory");
    if (tid < 64) { float t = 0.f;
#pragma unroll
      for (int ww = 0; ww < 8; ++ww) t += gr[ww * 64 + tid];
      modp[16384 + gks * 2048 + gcg * 64 + tid] = t; }
  }
  ST(6);
  asm volatile("s_waitcnt vmcnt(0)" : "+v"(dummy));
  ST_END(4);
}

DI unsigned wimg_src_chunk(int lane) { return (unsigned)(((lane & 7) ^ (((lane >> 4) & 3) << 1)) << 4); }
DI int wimg_rd(int lane, int t) { const int g = lane >> 4, q = (lane & 15) >> 2, r8 = 4 * (g & 1) + q; return (g >> 1) * 1024 + r8 * 128 + ((t ^ (r8 >> 1)) * 32) + (lane & 3) * 8; }

DI bf16x8 pack8(const f32x16& p, int s) {
  u32x4 r = {cvtpk(p[8 * s], p[8 * s + 1]), cvtpk(p[8 * s + 2], p[8 * s + 3]), cvtpk(p[8 * s + 4], p[8 * s + 5]), cvtpk(p[8 * s + 6], p[8 * s + 7])};
  return __builtin_bit_cast(bf16x8, r);
}
DI float rowmax16(const f32x16& p) {
  float a = fmaxf(fmaxf(p[0], p[1]), p[2]), b = fmaxf(fmaxf(p[3], p[4]), p[5]);
  a = fmaxf(fmaxf(a, p[6]), p[7]); b = fmaxf(fmaxf(b, p[8]), p[9]); a = fmaxf(fmaxf(a, p[10]), p[11]); b = fmaxf(fmaxf(b, p[12]), p[13]);
  a = fmaxf(fmaxf(a, p[14]), p[15]); a = fmaxf(a, b);
  auto rr = __builtin_amdgcn_permlane32_swap(__float_as_uint(a), __float_as_uint(a), false, false);
  return fmaxf(__uint_as_float(rr[0]), __uint_as_float(rr[1]));
}
DI bf16x8 plo(const u32x8& p) { return __builtin_bit_cast(bf16x8, __builtin_shufflevector(p, p, 0, 1, 2, 3)); }
DI bf16x8 phi(const u32x8& p) { return __builtin_bit_cast(bf16x8, __builtin_shufflevector(p, p, 4, 5, 6, 7)); }
#define ROWSUM(sel, p, acc, idx) __builtin_amdgcn_smfmac_f32_16x16x64_bf16(__builtin_bit_cast(bf16x8n, sel), __builtin_bit_cast(bf16x16n, p), acc, idx, 0, 0)

extern "C" __global__ __launch_bounds__(512, 2) void k3_attn(const bf16_t* hfm, const bf16_t* wq, const bf16_t* kvf, bf16_t* ofm, const bf16_t* wg, const bf16_t* wu,
                                                  const bf16_t* cvec, float* utp, const bf16_t* wo, const bf16_t* wd, const bf16_t* x,
                                                  const float* modp, float* modf STAMP_ARG) {
  extern __shared__ __attribute__((aligned(16))) char lds[];
  ST_DECL; ST(0);
  KEEP_S(hfm); KEEP_S(wq); KEEP_S(kvf); KEEP_S(ofm); KEEP_S(wg); KEEP_S(wu); KEEP_S(cvec); KEEP_S(utp); KEEP_S(wo); KEEP_S(wd); KEEP_S(x); KEEP_S(modp); KEEP_S(modf);
  ST(7);
  const int tid = threadIdx.x, lane = tid & 63, w = RFL(tid >> 6), wm = w & 3, wk = w >> 2;
  const int xcd = blockIdx.x & 7, bi = blockIdx.x >> 3, tm = (xcd >> 1) * 4 + (bi & 3), hp = (xcd & 1) * 8 + (bi >> 2);
  const unsigned lds0 = (unsigned)(uintptr_t)lds;
  const lds_cptr L = (lds_cptr)lds;
  const int hi = lane >> 5;
  const unsigned vlin = (unsigned)(lane * 16);
  constexpr int LW = 32768, LUT = 98304;
  const int rot = (bi >> 2) * 4, crot = bi & 7;
  u32x4 hf[32];
  const char* hb = (const char*)hfm + (size_t)(wk * 32 * 64 + tm * 4 + wm) * 1024;
#define HLOAD(i) GLOAD4(hf[i], hb + (size_t)((i) ^ rot) * 65536, vlin, 0)
#define KV_LD(c, slot, q) glds16((const char*)kvf + (((c) + crot) & 7) * 32768 + w * 4096 + (q) * 1024, vlin, lds0 + (slot) * 32768 + w * 4096 + (q) * 1024)
  const unsigned wvoff = (unsigned)((w * 8 + (lane >> 3)) * 2048 + hp * 128 + (((lane & 7) ^ (((lane >> 4) & 1) << 2)) << 4));
#define WLD(q) glds16((const char*)wq + ((q) ^ (rot >> 2)) * 64 * 2048, wvoff, lds0 + LW + ((q) * 8 + w) * 1024)
  WLD(0); WLD(1); WLD(8); WLD(9); HLOAD(0); HLOAD(1); HLOAD(2); HLOAD(3);
  u32x4 cf[8]; unsigned gpart;
  const unsigned cvoff = (unsigned)((lane >> 4) * 32 + (lane & 1) * 16), gvoff = (unsigned)(((lane >> 3) * 2048 + blockIdx.x * 8 + (lane & 7)) * 4);
#define CLOAD(J) GLOAD4(cf[J], (const char*)cvec + (xcd & 1) * 2048 + wk * 1024, cvoff, (J) * 128)
  f32x16 qa0 = {}, qa1 = {};
  {
    const int q = (lane & 15) >> 2, pp = lane & 3, nb = (lane >> 4) & 1;
    lds_cptr a0 = L + LW + wk * 65536 + hi * 1024 + q * 128 + (((q >> 1) & 1) * 64) + nb * 32 + pp * 8;
    lds_cptr a1 = L + LW + wk * 65536 + hi * 1024 + q * 128 + ((1 ^ ((q >> 1) & 1)) * 64) + nb * 32 + pp * 8;
    PIN2(a0, a1); PIN2(qa0, qa1);
    asm volatile("s_waitcnt vmcnt(4)\n\ts_barrier" ::: "memory");
    ST(1);
    bf16x8 w0 = ldtr2(a0, a0 + 512), w1 = ldtr2(a1, a1 + 512);
#define QSTEP(i, N, B, LOADS) do { asm volatile("s_waitcnt vmcnt(" #N ")" : "+v"(hf[i])); if ((i) == 23) WAITL(); if (B) BAR(); \
    bf16x8 n0 = w0, n1 = w1; if ((i) < 31) { n0 = ldtr2(a0 + ((i) + 1) * 2048, a0 + ((i) + 1) * 2048 + 512); n1 = ldtr2(a1 + ((i) + 1) * 2048, a1 + ((i) + 1) * 2048 + 512); } SBAR(); \
    qa0 = MFMA32(w0, __builtin_bit_cast(bf16x8, hf[i]), qa0); PIN(qa0); SBAR(); LOADS; qa1 = MFMA32(w1, __builtin_bit_cast(bf16x8, hf[i]), qa1); PIN(qa1); SBAR(); w0 = n0; w1 = n1; } while (0)
    QSTEP(0, 3, 0, HLOAD(4); WLD(2); HLOAD(5));
    QSTEP(1, 5, 0, WLD(3); HLOAD(6); WLD(10));
    QSTEP(2, 7, 0, WLD(11); HLOAD(7); HLOAD(8));
    QSTEP(3, 9, 0, HLOAD(9));
    QSTEP(4, 9, 0, HLOAD(10); WLD(4));
    QSTEP(5, 9, 0, HLOAD(11));
    QSTEP(6, 8, 0, WLD(5); HLOAD(12));
    QSTEP(7, 7, 1, WLD(12));
    QSTEP(8, 7, 0, HLOAD(13); WLD(13));
    QSTEP(9, 8, 0, HLOAD(14));
    QSTEP(10, 8, 0, HLOAD(15); HLOAD(16));
    QSTEP(11, 8, 0, HLOAD(17));
    QSTEP(12, 7, 0, HLOAD(18); WLD(6));
    QSTEP(13, 7, 0, HLOAD(19));
    QSTEP(14, 6, 0, WLD(7); HLOAD(20));
    QSTEP(15, 7, 1, WLD(14));
    QSTEP(16, 7, 0, HLOAD(21); WLD(15));
    QSTEP(17, 8, 0, HLOAD(22));
    QSTEP(18, 8, 0, HLOAD(23); HLOAD(24));
    QSTEP(19, 8, 0, HLOAD(25));
    QSTEP(20, 7, 0, KV_LD(0, 0, 0); HLOAD(26));
    QSTEP(21, 7, 0, HLOAD(27));
    QSTEP(22, 6, 0, KV_LD(0, 0, 1); HLOAD(28));
    QSTEP(23, 7, 1, HLOAD(29));
    QSTEP(24, 7, 0, KV_LD(0, 0, 2); HLOAD(30));
    QSTEP(25, 8, 0, HLOAD(31); KV_LD(0, 0, 3));
    QSTEP(26, 8, 0, CLOAD(0); CLOAD(1));
    QSTEP(27, 9, 0, CLOAD(2); CLOAD(3));
    QSTEP(28, 9, 0, CLOAD(4); CLOAD(5));
    QSTEP(29, 10, 0, CLOAD(6); CLOAD(7));
    QSTEP(30, 10, 0, GLOAD1(gpart, modp + 16384, gvoff));
    QSTEP(31, 10, 0, (void)0);
  }
  ST(2);
  bf16x8 qfa0, qfa1, qfb0, qfb1;
  {
    lds_ptr xw = (lds_ptr)lds + (w < 6 ? LUT + w * 8192 : 65536 + (w - 6) * 8192) + lane * 16;
    lds_cptr xr = L + ((w ^ 4) < 6 ? LUT + (w ^ 4) * 8192 : 65536 + ((w ^ 4) - 6) * 8192) + lane * 16;
#pragma unroll
    for (int i = 0; i < 4; ++i) { *(LAS f32x4*)(xw + i * 1024) = (f32x4){qa0[4 * i], qa0[4 * i + 1], qa0[4 * i + 2], qa0[4 * i + 3]};
      *(LAS f32x4*)(xw + 4096 + i * 1024) = (f32x4){qa1[4 * i], qa1[4 * i + 1], qa1[4 * i + 2], qa1[4 * i + 3]}; }
    KV_LD(1, 1, 0); KV_LD(1, 1, 1); KV_LD(1, 1, 2); KV_LD(1, 1, 3);
    asm volatile("s_waitcnt vmcnt(13) lgkmcnt(0)\n\ts_barrier" ::: "memory");
#pragma unroll
    for (int i = 0; i < 4; ++i) { const f32x4 o0 = *(const LAS f32x4*)(xr + i * 1024), o1 = *(const LAS f32x4*)(xr + 4096 + i * 1024);
#pragma unroll
      for (int j = 0; j < 4; ++j) { qa0[4 * i + j] += o0[j]; qa1[4 * i + j] += o1[j]; } }
    qfa0 = pack8(qa0, 0); qfa1 = pack8(qa0, 1); qfb0 = pack8(qa1, 0); qfb1 = pack8(qa1, 1);
  }
  u32x4 sel;
  { const unsigned on = (lane & 2) ? 0u : 0x3f803f80u; const bool od = (lane >> 2) & 1; sel = (u32x4){od ? 0u : on, od ? 0u : on, od ? on : 0u, od ? on : 0u}; }
  const int sidx = (lane & 1) ? 0xEEEE : 0x4444;
  f32x16 oa = {}, ob = {}; f32x4 la = {}, lb = {};
  const lds_cptr kv0 = L + wk * 16384 + lane * 16;
  bf16x8 kf0 = ld128(kv0), kf1 = ld128(kv0 + 1024), vf0 = ld128(kv0 + 2048), vf1 = vf0;
  f32x16 sA, sB, na, nb;
  {
    const f32x16 z = {};
    sA = MFMA32(kf0, qfa0, z); sB = MFMA32(kf0, qfb0, z); sA = MFMA32(kf1, qfa1, sA); sB = MFMA32(kf1, qfb1, sB);
    const float ma = rowmax16(sA), mb = rowmax16(sB);
#pragma unroll
    for (int r = 0; r < 16; ++r) { sA[r] -= ma; na[r] = -ma; nb[r] = -mb; }
    PIN2(na, nb);
  }
  u32x8 pA, pB = {0, 0, 0, 0, 0, 0, 0, 0};
  const int ub = bi < 28 ? bi : bi - 28, ucg = (xcd >> 1) * 14 + (ub % 14);
  const char* usrc = (const char*)(ub < 14 ? wg : wu) + ucg * 128 + (size_t)((xcd & 1) * 512 + wm * 8) * 7168;
  const unsigned uvoff = (unsigned)((lane >> 3) * 7168) + wimg_src_chunk(lane);
#define SIDE(cw, slot, k) do { if (!wk) { if ((cw) < 6) { const char* s_ = (const char*)kvf + (((cw) + 2 + crot) & 7) * 32768 + wm * 8192 + (k) * 2048; const unsigned d_ = lds0 + (slot) * 32768 + wm * 8192 + (k) * 2048; \
        glds16(s_, vlin, d_); glds16(s_ + 1024, vlin, d_ + 1024); } } \
    else if ((cw) < 4) glds16(usrc + (size_t)((cw) * 4 + (k)) * (32 * 7168), uvoff, lds0 + LUT + (((cw) * 4 + (k)) * 4 + wm) * 1024); \
    else if ((k) == 0) { if ((cw) == 4) TOUCH255((const char*)wd + (blockIdx.x * 4 + wm) * 7168, (unsigned)((lane < 56 ? lane : 55) * 128)); \
      else if ((cw) < 7) TOUCH255((const char*)wo + ((bi * 4 + wm) * 2 + (cw) - 5) * 8192, (unsigned)(lane * 128)); } \
    else if ((k) == 3 && (cw) == 5) TOUCH255((const char*)x + xcd * 524288 + (bi * 4 + wm) * 4096, (unsigned)((lane & 31) * 128)); } while (0)
#define EX(S, r) S[r] = __builtin_amdgcn_exp2f(S[r])
#define CV(P, S, r) P[(r) >> 1] = cvtpk(S[r], S[(r) + 1])
#define HS(SC, SN, PP, PC, q0, q1, nm, oac, lac, L0, L1, L2, L3, G) do { \
    SN = MFMA32(kf0, q0, nm); SBAR(); L0; EX(SC, 0); EX(SC, 1); EX(SC, 2); EX(SC, 3); PIN(SC); SBAR(); \
    SN = MFMA32(kf1, q1, SN); SBAR(); L1; EX(SC, 4); EX(SC, 5); CV(PC, SC, 0); CV(PC, SC, 2); EX(SC, 6); PIN2(SC, PC); SBAR(); \
    oac = MFMA32(vf0, plo(PP), oac); SBAR(); L2; EX(SC, 7); EX(SC, 8); CV(PC, SC, 4); EX(SC, 9); CV(PC, SC, 6); PIN2(SC, PC); SBAR(); \
    oac = MFMA32(vf1, phi(PP), oac); SBAR(); L3; EX(SC, 10); EX(SC, 11); CV(PC, SC, 8); EX(SC, 12); CV(PC, SC, 10); PIN2(SC, PC); SBAR(); \
    lac = ROWSUM(sel, PP, lac, sidx); SBAR(); G; EX(SC, 13); EX(SC, 14); EX(SC, 15); CV(PC, SC, 12); CV(PC, SC, 14); PIN(PC); SBAR(); } while (0)
#define STEP(pc, pn, G0, G1) do { HS(sA, sB, pB, pA, qfb0, qfb1, nb, ob, lb, kf0 = ld128(pn), kf1 = ld128((pn) + 1024), vf0 = ld128((pc) + 2048), vf1 = ld128((pc) + 3072), G0); \
    HS(sB, sA, pA, pB, qfa0, qfa1, na, oa, la, (void)0, (void)0, (void)0, (void)0, G1); } while (0)
  asm volatile("s_waitcnt vmcnt(4)" : "+v"(cf[0]), "+v"(cf[1]), "+v"(cf[2]), "+v"(cf[3]), "+v"(cf[4]), "+v"(cf[5]), "+v"(cf[6]), "+v"(cf[7]), "+v"(gpart) :: "memory");
  ST(3);
  int sl = 0;
#pragma unroll 1
  for (int c = 0; c < 8; ++c) {
    const int s1 = sl == 2 ? 0 : sl + 1, s2 = s1 == 2 ? 0 : s1 + 1;
    const lds_cptr pc = kv0 + sl * 32768, pn = kv0 + s1 * 32768;
    const unsigned cp = (unsigned)(c - 1);
    __builtin_amdgcn_s_setprio(2); STEP(pc, pc + 4096, SIDE(cp, s1, 2), SIDE(cp, s1, 3));
    __builtin_amdgcn_s_setprio(1); STEP(pc + 4096, pc + 8192, (void)0, (void)0);
    __builtin_amdgcn_s_setprio(0); STEP(pc + 8192, pc + 12288, (void)0, (void)0);
    if (c < 7) { if (!wk || c == 0 || c == 5) WAITV(0); BAR(); }
    __builtin_amdgcn_s_setprio(3);
    if (c == 5) {
      f32x4 ua = {};
      const lds_cptr wr = L + LUT + wk * 32768 + wimg_rd(lane, wm);
#pragma unroll
      for (int J = 0; J < 8; ++J) ua = MFMA16(ldtr2(wr + J * 4096, wr + J * 4096 + 2048), __builtin_bit_cast(bf16x8, cf[J]), ua);
      if ((lane & 15) < 2 && bi < 28) *(f32x4*)(utp + ((((xcd & 1) * 2 + wk) * 28 + (ucg >> 1)) * 4 + (ub < 14 ? 0 : 2) + (lane & 1)) * 128 + (ucg & 1) * 64 + 16 * wm + 4 * (lane >> 4)) = ua;
      const int gi = blockIdx.x * 8 + (lane & 7);
      const float t = sum4g(dpp_add<0x128>(__uint_as_float(gpart)));
      if (tid < 8) modf[(gi < 1024 ? 2048 : 4096) + gi] = rbf(t);
      if (CODEPF && bi >= 28 && w == 7) { const char *c0, *c1; SYM_ADDR(c0, k4_oproj); SYM_ADDR(c1, k7_end); TOUCH255(c0, CODE_OFF(c0, c1, bi - 28, lane)); }
    }
    SBAR();
    STEP(pc + 12288, pn, SIDE(c, s2, 0), SIDE(c, s2, 1));
    sl = s1;
  }
  ob = MFMA32(vf0, plo(pB), ob); ob = MFMA32(vf1, phi(pB), ob); lb = ROWSUM(sel, pB, lb, sidx);
  ST(4);
  f32x16 oacc; float lsum; bool bad;
  {
    const f32x16 snd = wk ? oa : ob; oacc = wk ? ob : oa;
    const float l_a = la[0] + la[1], l_b = lb[0] + lb[1];
    const float ls = wk ? l_a : l_b, lk = wk ? l_b : l_a, ns = wk ? na[0] : nb[0], nk = wk ? nb[0] : na[0];
    lds_ptr xb = (lds_ptr)lds + LUT;
#pragma unroll
    for (int i = 0; i < 4; ++i) *(LAS f32x4*)(xb + w * 8192 + i * 1024 + lane * 16) = (f32x4){snd[4 * i], snd[4 * i + 1], snd[4 * i + 2], snd[4 * i + 3]};
    *(LAS f32x2*)(xb + w * 8192 + 4096 + lane * 8) = (f32x2){ls, ns};
    asm volatile("s_waitcnt lgkmcnt(0)\n\ts_barrier" ::: "memory");
    const f32x2 pl = *(const LAS f32x2*)(xb + (w ^ 4) * 8192 + 4096 + lane * 8);
    const float nmin = fminf(nk, pl[1]), fk = __builtin_amdgcn_exp2f(nmin - nk), fp = __builtin_amdgcn_exp2f(nmin - pl[1]);
    lsum = lk * fk + pl[0] * fp;
    bad = !(lk < 1.0e12f) || !(pl[0] < 1.0e12f);
#pragma unroll
    for (int i = 0; i < 4; ++i) { const f32x4 o = *(const LAS f32x4*)(xb + (w ^ 4) * 8192 + i * 1024 + lane * 16);
#pragma unroll
      for (int j = 0; j < 4; ++j) oacc[4 * i + j] = oacc[4 * i + j] * fk + o[j] * fp; }
  }
#pragma unroll
  for (int r = 0; r < 16; ++r) bad = bad || !(fabsf(oacc[r]) < 3.0e38f);
  if (__any(bad)) {
    const bf16x8 qf0 = wk ? qfb0 : qfa0, qf1 = wk ? qfb1 : qfa1;
    float m = -INFINITY; lsum = 0.f;
#pragma unroll
    for (int r = 0; r < 16; ++r) oacc[r] = 0.f;
    const bf16_t* g = kvf + lane * 8;
    for (int t = 0; t < 64; ++t) {
      const f32x16 z = {};
      f32x16 s = MFMA32(*(const bf16x8*)(g + t * 2048), qf0, z); s = MFMA32(*(const bf16x8*)(g + t * 2048 + 512), qf1, s);
      const float mn = fmaxf(m, rowmax16(s)), al = __builtin_amdgcn_exp2f(m - mn); m = mn;
#pragma unroll
      for (int r = 0; r < 16; ++r) { s[r] = __builtin_amdgcn_exp2f(s[r] - mn); oacc[r] *= al; }
      u32x8 p;
#pragma unroll
      for (int r = 0; r < 16; r += 2) CV(p, s, r);
      f32x4 lt = {}; lt = ROWSUM(sel, p, lt, sidx);
      lsum = lsum * al + (lt[0] + lt[1]);
      oacc = MFMA32(*(const bf16x8*)(g + t * 2048 + 1024), plo(p), oacc); oacc = MFMA32(*(const bf16x8*)(g + t * 2048 + 1536), phi(p), oacc);
    }
  }
  const float rl = __builtin_amdgcn_rcpf(lsum);
#pragma unroll
  for (int r = 0; r < 16; ++r) oacc[r] *= rl;
  {
    const int m16 = tm * 8 + wm * 2 + ((lane >> 4) & 1), J = hp * 2 + wk;
    bf16_t* op = ofm + ((size_t)(J * 128 + m16) * 64 + (lane & 15) + 32 * hi) * 8;
    store16(op, __builtin_bit_cast(u32x4, pack8(oacc, 0))); store16(op + 128, __builtin_bit_cast(u32x4, pack8(oacc, 1)));
  }
  ST(6);
  ST_END(5);
}

#define GLOAD2(dst, sbase, voff, imm) asm volatile("global_load_dwordx2 %0, %1, %2 offset:%3" : "=v"(dst) : "v"(voff), "s"(sbase), "n"(imm) : "memory")
extern "C" __global__ __launch_bounds__(512, 2) void k4_oproj(const bf16_t* ofm, const bf16_t* wo, const bf16_t* x, const float* modf, bf16_t* x1fm, bf16_t* yfm, f32x2* stat,
                                                   const float* utp, float* utf, unsigned* flag STAMP_ARG) {
  extern __shared__ __attribute__((aligned(16))) char lds[];
  ST_DECL; ST(0);
  KEEP_S(ofm); KEEP_S(wo); KEEP_S(x); KEEP_S(modf); KEEP_S(x1fm); KEEP_S(yfm); KEEP_S(stat); KEEP_S(utp); KEEP_S(utf); KEEP_S(flag);
  ST(7);
  const int tid = threadIdx.x, lane = tid & 63, w = RFL(tid >> 6), wm = w & 3, wk = w >> 2;
  const int xcd = blockIdx.x & 7, bi = blockIdx.x >> 3, tm = xcd * 2 + (bi & 1), tn = bi >> 1;
  const unsigned lds0 = (unsigned)(uintptr_t)lds;
  const lds_cptr L = (lds_cptr)lds;
  const unsigned vlin = (unsigned)(lane * 16);
  u32x4 af[16][2];
  const char* ab = (const char*)ofm + (size_t)(wk * 16 * 128 + tm * 8 + wm * 2) * 1024;
#define AL(i, h) GLOAD4(af[i][h], ab + (size_t)((i) ^ tn) * 131072, vlin, (h) * 1024)
  const unsigned wvoff = (unsigned)(((w ^ ((tn & 1) << 2)) * 8 + (lane >> 3)) * 2048 + tn * 128) + wimg_src_chunk(lane);
#define OWL(q) glds16((const char*)wo + ((q) ^ (tn >> 1)) * 64 * 2048, wvoff, lds0 + ((q) * 8 + w) * 1024)
  AL(0, 0); AL(0, 1); OWL(0); OWL(1); OWL(8); OWL(9); AL(1, 0); AL(1, 1);
  const int g = lane >> 4, tok = tm * 128 + wm * 32 + wk * 16 + (lane & 15);
  const unsigned xvoff = (unsigned)((lane >> 3) * 2048 + (((lane & 7) ^ (lane >> 3)) << 4)), mvoff = (unsigned)(((lane >> 4) & 1) * 8192 + (lane & 15) * 16);
#define OEL(k) do { if ((k) < 2) glds16((const char*)x + (size_t)(tm * 128 + wm * 32 + wk * 16 + (k) * 8) * 2048 + tn * 128, xvoff, lds0 + 131072 + w * 4096 + (k) * 1024); \
    else glds16(modf + 2048 + tn * 64, mvoff, lds0 + 131072 + w * 4096 + 2048); } while (0)
  unsigned up[4]; const unsigned uvoff = (unsigned)(((blockIdx.x * 8 + w) * 7 + (lane < 7 ? lane : 0)) * 4);
#define UL(k) GLOAD1(up[k], utp + (k) * 14336, uvoff)
  f32x4 acc[2][4] = {};
  {
    lds_cptr a[4];
#pragma unroll
    for (int t = 0; t < 4; ++t) { a[t] = L + wk * 65536 + wimg_rd(lane, t); PIN(a[t]); PIN2(acc[0][t], acc[1][t]); }
    asm volatile("s_waitcnt vmcnt(2)\n\ts_barrier" ::: "memory");
    ST(1);
    bf16x8 wf[4], wn[4];
#pragma unroll
    for (int t = 0; t < 4; ++t) wf[t] = ldtr2(a[t], a[t] + 2048);
#define OMM(i, t) do { acc[0][t] = MFMA16(wf[t], __builtin_bit_cast(bf16x8, af[i][0]), acc[0][t]); acc[1][t] = MFMA16(wf[t], __builtin_bit_cast(bf16x8, af[i][1]), acc[1][t]); PIN2(acc[0][t], acc[1][t]); SBAR(); } while (0)
#define OSTEP(i, N, B, L0, L1, L2, L3) do { asm volatile("s_waitcnt vmcnt(" #N ")" : "+v"(af[i][0]), "+v"(af[i][1])); if (B) BAR(); \
    _Pragma("unroll") for (int t = 0; t < 4; ++t) wn[t] = (i) < 15 ? ldtr2(a[t] + ((i) + 1) * 4096, a[t] + ((i) + 1) * 4096 + 2048) : wf[t]; SBAR(); \
    OMM(i, 0); L0; OMM(i, 1); L1; OMM(i, 2); L2; OMM(i, 3); L3; _Pragma("unroll") for (int t = 0; t < 4; ++t) wf[t] = wn[t]; } while (0)
    OSTEP(0, 6, 0, AL(2, 0); AL(2, 1), OWL(2), OWL(3); OWL(10), OWL(11));
    OSTEP(1, 6, 0, AL(3, 0), AL(3, 1), AL(4, 0), AL(4, 1));
    OSTEP(2, 8, 0, OWL(4), AL(5, 0), AL(5, 1), OWL(5));
    OSTEP(3, 6, 1, OWL(12), AL(6, 0), AL(6, 1), (void)0);
    OSTEP(4, 7, 0, OWL(13), AL(7, 0), AL(7, 1), (void)0);
    OSTEP(5, 7, 0, OWL(6), AL(8, 0), AL(8, 1), (void)0);
    OSTEP(6, 6, 0, OWL(7), AL(9, 0), AL(9, 1), (void)0);
    OSTEP(7, 6, 1, OWL(14), AL(10, 0), AL(10, 1), (void)0);
    OSTEP(8, 6, 0, OWL(15), AL(11, 0), AL(11, 1), (void)0);
    OSTEP(9, 6, 0, OEL(0), AL(12, 0), AL(12, 1), (void)0);
    OSTEP(10, 6, 0, OEL(1), AL(13, 0), AL(13, 1), (void)0);
    OSTEP(11, 6, 1, OEL(2), AL(14, 0), AL(14, 1), (void)0);
    OSTEP(12, 6, 0, UL(0), AL(15, 0), AL(15, 1), (void)0);
    OSTEP(13, 6, 0, UL(1), UL(2), UL(3), (void)0);
    OSTEP(14, 6, 0, (void)0, (void)0, (void)0, (void)0);
    OSTEP(15, 3, 0, (void)0, (void)0, (void)0, (void)0);
  }
  ST(2);
  lds_ptr xb = (lds_ptr)lds + 131072;
  u32x2 xr[4]; f32x4 gav[4], cbv[4];
  asm volatile("s_waitcnt vmcnt(0)" : "+v"(up[0]), "+v"(up[1]), "+v"(up[2]), "+v"(up[3]) :: "memory");
  if (lane == 8) flag[(blockIdx.x * 8 + w) * 32] = 0u;
  if (lane < 7) utf[uvoff >> 2] = (__uint_as_float(up[0]) + __uint_as_float(up[1])) + (__uint_as_float(up[2]) + __uint_as_float(up[3]));
#pragma unroll
  for (int t = 0; t < 4; ++t) { const lds_cptr e = xb + w * 4096;
    xr[t] = *(const LAS u32x2*)(e + (lane & 15) * 128 + (((2 * t + (g >> 1)) ^ (lane & 7)) << 4) + (g & 1) * 8);
    gav[t] = *(const LAS f32x4*)(e + 2048 + t * 64 + g * 16); cbv[t] = *(const LAS f32x4*)(e + 2048 + 256 + t * 64 + g * 16); }
  asm volatile("s_waitcnt lgkmcnt(0)" : "+v"(xr[0]), "+v"(xr[1]), "+v"(xr[2]), "+v"(xr[3]), "+v"(gav[0]), "+v"(gav[1]), "+v"(gav[2]), "+v"(gav[3]),
               "+v"(cbv[0]), "+v"(cbv[1]), "+v"(cbv[2]), "+v"(cbv[3]) :: "memory");
#pragma unroll
  for (int t = 0; t < 4; ++t) *(LAS f32x4*)(xb + w * 4096 + t * 1024 + lane * 16) = wk ? acc[0][t] : acc[1][t];
  asm volatile("s_waitcnt lgkmcnt(0)\n\ts_barrier" ::: "memory");
  float v[16]; float s = 0.f;
#pragma unroll
  for (int t = 0; t < 4; ++t) {
    const f32x4 o = *(const LAS f32x4*)(xb + (w ^ 4) * 4096 + t * 1024 + lane * 16) + (wk ? acc[1][t] : acc[0][t]), ga = gav[t];
    v[4 * t] = rbf(bflo(xr[t][0]) + ga[0] * o[0]); v[4 * t + 1] = rbf(bfhi(xr[t][0]) + ga[1] * o[1]);
    v[4 * t + 2] = rbf(bflo(xr[t][1]) + ga[2] * o[2]); v[4 * t + 3] = rbf(bfhi(xr[t][1]) + ga[3] * o[3]);
    s += (v[4 * t] + v[4 * t + 1]) + (v[4 * t + 2] + v[4 * t + 3]);
  }
  const size_t fo = ((size_t)(tn * 2 * 128 + (tok >> 4)) * 64 + lane) * 8;
#pragma unroll
  for (int jj = 0; jj < 2; ++jj) {
    const float* p = v + 8 * jj; const f32x4 c0 = cbv[2 * jj], c1 = cbv[2 * jj + 1];
    store16(yfm + fo + jj * 65536, (u32x4){cvtpk(p[0] * c0[0], p[1] * c0[1]), cvtpk(p[2] * c0[2], p[3] * c0[3]), cvtpk(p[4] * c1[0], p[5] * c1[1]), cvtpk(p[6] * c1[2], p[7] * c1[3])});
    store16(x1fm + fo + jj * 65536, (u32x4){cvtpk(p[0], p[1]), cvtpk(p[2], p[3]), cvtpk(p[4], p[5]), cvtpk(p[6], p[7])});
  }
  const float mw = sum4g(s) * (1.f / 64.f); float q = 0.f;
#pragma unroll
  for (int j = 0; j < 16; ++j) { const float d = v[j] - mw; q += d * d; }
  q = sum4g(q);
  if (g == 0) stat[((tok >> 4) * 16 + tn) * 16 + (lane & 15)] = (f32x2){mw, q};
  ST(6);
  ST_END(3);
}

extern "C" __global__ __launch_bounds__(512, 2) void k5_gateup(const bf16_t* yfm, const bf16_t* wg, const bf16_t* wu, const f32x2* stat, const float* utp, bf16_t* actfm STAMP_ARG) {
  extern __shared__ __attribute__((aligned(16))) char lds[];
  ST_DECL; ST(0);
  KEEP_S(yfm); KEEP_S(wg); KEEP_S(wu); KEEP_S(stat); KEEP_S(utp); KEEP_S(actfm);
  ST(7);
  const int tid = threadIdx.x, lane = tid & 63, w = RFL(tid >> 6), wm = w & 3, wn = w >> 2;
  const int xcd = blockIdx.x & 7, bi = blockIdx.x >> 3, tm = (xcd & 1) * 4 + (bi & 3), tn = (xcd >> 1) * 7 + (bi >> 2);
  const unsigned lds0 = (unsigned)(uintptr_t)lds;
  const lds_cptr L = (lds_cptr)lds;
  const char* sA = (const char*)yfm + (size_t)(tm * 16 + 2 * w) * 1024;
  const char* sB = (const char*)(w < 4 ? wg : wu) + (size_t)(tn * 128 + ((w >> 1) & 1) * 64) * 2 + (size_t)(16 * (w & 1)) * 7168;
  const unsigned vA = (unsigned)(lane * 16), vB = (unsigned)((lane >> 3) * 7168) + wimg_src_chunk(lane);
#define K5_ISSUE(s, slot) do { const unsigned d_ = lds0 + (slot) * 32768 + w * 2048; \
    glds16(sA + (size_t)(s) * 131072, vA, d_); glds16(sA + (size_t)(s) * 131072 + 1024, vA, d_ + 1024); \
    glds16(sB + (size_t)(s) * 32 * 7168, vB, d_ + 16384); glds16(sB + (size_t)(s) * 32 * 7168 + 8 * 7168, vB, d_ + 16384 + 1024); } while (0)
  { const char* sS = (const char*)stat + tm * 32768 + w * 4096; const unsigned d_ = lds0 + 3 * 32768 + w * 4096;
    glds16(sS, vA, d_); glds16(sS + 1024, vA, d_ + 1024); glds16(sS + 2048, vA, d_ + 2048); glds16(sS + 3072, vA, d_ + 3072); }
  glds16((const char*)utp + tn * 2048 + (w & 1) * 1024, vA, lds0 + 131072 + w * 1024);
  K5_ISSUE(0, 0); K5_ISSUE(1, 1);
  lds_cptr ra[2], rb[2][4];
#pragma unroll
  for (int h = 0; h < 2; ++h) { ra[h] = L + h * 65536 + wm * 4096 + lane * 16; PIN(ra[h]);
#pragma unroll
    for (int t = 0; t < 4; ++t) { rb[h][t] = L + h * 65536 + 16384 + wn * 4096 + wimg_rd(lane, t); PIN(rb[h][t]); } }
  asm volatile("s_waitcnt vmcnt(8)\n\ts_barrier" ::: "memory");
  K5_ISSUE(2, 2);
  f32x4 ag[4][4], au[4][4]; float rs[4];
  {
    float sd[4], nmean[4];
#pragma unroll
    for (int m = 0; m < 4; ++m) {
      const lds_cptr sp = L + 3 * 32768 + (wm * 4 + m) * 2048 + (lane >> 4) * 512 + (lane & 15) * 8;
      f32x2 st[4];
#pragma unroll
      for (int j = 0; j < 4; ++j) st[j] = *(const LAS f32x2*)(sp + j * 128);
      const float mean = sum4g((st[0][0] + st[1][0]) + (st[2][0] + st[3][0])) * (1.f / 16.f); float q = 0.f;
#pragma unroll
      for (int j = 0; j < 4; ++j) { const float d = st[j][0] - mean; q += st[j][1] + 64.f * d * d; }
      const float var = sum4g(q) * (1.f / D_) + LN_EPS;
      rs[m] = __builtin_amdgcn_rsqf(var); sd[m] = var * rs[m]; nmean[m] = -mean;
    }
#pragma unroll
    for (int t = 0; t < 4; ++t) { const LAS float* p = (const LAS float*)(L + 131072) + wn * 64 + t * 16 + 4 * (lane >> 4);
      const f32x4 ug = *(const LAS f32x4*)(p), tg = *(const LAS f32x4*)(p + 128), uu = *(const LAS f32x4*)(p + 256), tu = *(const LAS f32x4*)(p + 384);
#pragma unroll
      for (int m = 0; m < 4; ++m) { ag[m][t] = tg * sd[m] + ug * nmean[m]; au[m][t] = tu * sd[m] + uu * nmean[m]; PIN2(ag[m][t], au[m][t]); } }
  }
  asm volatile("s_waitcnt vmcnt(8) lgkmcnt(0)\n\ts_barrier" ::: "memory");
  ST(1);
  if (wn) BAR();
#define K5_STEP(slot, sn, LAST) do { \
    bf16x8 xa[4], wgf[4], wuf[4]; \
    _Pragma("unroll") for (int t = 0; t < 4; ++t) { const lds_cptr p_ = rb[(slot) >> 1][t] + ((slot) & 1) * 32768; wgf[t] = ldtr2(p_, p_ + 2048); wuf[t] = ldtr2(p_ + 8192, p_ + 8192 + 2048); } \
    _Pragma("unroll") for (int m = 0; m < 4; ++m) xa[m] = ld128(ra[(slot) >> 1] + ((slot) & 1) * 32768 + m * 1024); \
    if ((sn) < 32) { K5_ISSUE(sn, ((slot) + 3) & 3); WAITV(8); } else WAITV(0); \
    asm volatile("s_waitcnt lgkmcnt(0)\n\ts_barrier" ::: "memory"); SBAR(); \
    __builtin_amdgcn_s_setprio(1); \
    _Pragma("unroll") for (int m = 0; m < 4; ++m) _Pragma("unroll") for (int t = 0; t < 4; ++t) { ag[m][t] = MFMA16(wgf[t], xa[m], ag[m][t]); au[m][t] = MFMA16(wuf[t], xa[m], au[m][t]); } \
    __builtin_amdgcn_s_setprio(0); SBAR(); \
    if (!(LAST)) BAR(); } while (0)
#pragma unroll 1
  for (int s = 0; s < 32; s += 4) {
    K5_STEP(0, s + 3, false);
    K5_STEP(1, s + 4, false);
    K5_STEP(2, s + 5, false);
    K5_STEP(3, s + 6, s == 28 && wn);
  }
  ST(2);
#pragma unroll
  for (int m = 0; m < 4; ++m) {
    const float r2 = rs[m] * rs[m], k0 = -2.302208198f * rs[m], k1 = (0.044715f * k0) * r2, iv = __builtin_amdgcn_rcpf(r2);
#pragma unroll
    for (int jj = 0; jj < 2; ++jj) {
      u32x4 o;
#pragma unroll
      for (int e = 0; e < 4; ++e) {
        const f32x4 a4 = ag[m][2 * jj + (e >> 1)], u4 = au[m][2 * jj + (e >> 1)];
        const f32x2 a = {a4[2 * (e & 1)], a4[2 * (e & 1) + 1]}, u = {u4[2 * (e & 1)], u4[2 * (e & 1) + 1]};
        const f32x2 z = a * ((a * a) * k1 + k0);
        const f32x2 ex = {__builtin_amdgcn_exp2f(z[0]), __builtin_amdgcn_exp2f(z[1])};
        const f32x2 dn = ex * iv + iv;
        const f32x2 rc = {__builtin_amdgcn_rcpf(dn[0]), __builtin_amdgcn_rcpf(dn[1])};
        const f32x2 r = (a * u) * rc;
        o[e] = cvtpk(r[0], r[1]);
      }
      store16(actfm + ((size_t)((tn * 4 + wn * 2 + jj) * 128 + tm * 16 + wm * 4 + m) * 64 + lane) * 8, o);
      SBAR();
    }
  }
  ST(6);
  ST_END(3);
}

extern "C" __global__ __launch_bounds__(512, 2) void k6_down(const bf16_t* actfm, const bf16_t* wd, const bf16_t* x1fm, const float* modf, bf16_t* out, unsigned* flag, float* part STAMP_ARG) {
  extern __shared__ __attribute__((aligned(16))) char lds[];
  ST_DECL; ST(0);
  KEEP_S(actfm); KEEP_S(wd); KEEP_S(x1fm); KEEP_S(modf); KEEP_S(out); KEEP_S(flag); KEEP_S(part);
  ST(7);
  const int tid = threadIdx.x, lane = tid & 63, w = RFL(tid >> 6), wm = w & 3, wk = w >> 2;
  const int xcd = blockIdx.x & 7, bi = blockIdx.x >> 3, z = xcd & 1, tm = (xcd >> 1) * 4 + (bi & 3), tn = (bi >> 2) * 2 + z;
  const unsigned lds0 = (unsigned)(uintptr_t)lds;
  const lds_cptr L = (lds_cptr)lds;
  constexpr int N1 = 20, KMID = N1 * 64, K2 = 3584 - KMID;
  const unsigned vW = (unsigned)((lane >> 3) * 2048) + wimg_src_chunk(lane), vA = (unsigned)(lane * 16);
  const char* w1 = (const char*)wd + (size_t)(z * K2 + w * 8) * 2048;
  const char* a1 = (const char*)actfm + ((size_t)(z * (K2 / 32) + wk) * 128 + tm * 8 + wm * 2) * 1024;
  const char* w2 = (const char*)wd + (size_t)(KMID + w * 16) * 2048 + tn * 128;
  const char* a2 = (const char*)actfm + ((size_t)(KMID / 32 + 2 * wk) * 128 + tm * 8 + wm * 2) * 1024;
  u32x4 ar[4][4];
#define D1W(u, g) glds16(w1 + 64 * 2048 + (tn ^ (g)) * 128, vW, lds0 + (u) * 16384 + (g) * 8192 + w * 1024)
#define D1A(u, h) GLOAD4(ar[u][h], a1, vA, (h) * 1024)
#define D1NEXT() do { w1 += 64 * 2048; a1 += 2 * 131072; } while (0)
#define D2W(u, j, h) glds16(w2 + ((j) * 128 + (h) * 8) * 2048, vW, lds0 + (u) * 16384 + w * 2048 + (h) * 1024)
#define D2A(u, j, k) GLOAD4(ar[u][k], a2 + (size_t)((j) * 4 + ((k) >> 1)) * 131072, vA, ((k) & 1) * 1024)
  w1 -= 64 * 2048; D1W(0, 0); D1W(0, 1); w1 += 64 * 2048;
  D1W(1, 0); D1W(1, 1); D1A(0, 0); D1A(0, 1); D1NEXT(); D1W(2, 0); D1W(2, 1); D1A(1, 0); D1A(1, 1); D1NEXT(); D1W(3, 0); D1W(3, 1); D1A(2, 0); D1A(2, 1);
  if (CODEPF && bi < 2 && w < 3) { const char *c0, *c1; SYM_ADDR(c0, k6_down); SYM_ADDR(c1, k7_end); TOUCH255(c0, CODE_OFF(c0, c1, w, lane)); }
  f32x4 acc[2][8] = {};
  lds_cptr a[4], a2l[4];
#pragma unroll
  for (int t = 0; t < 4; ++t) { a[t] = L + wk * 4096 + wimg_rd(lane, t); a2l[t] = a[t] + wk * 4096; PIN2(a[t], a2l[t]); }
#pragma unroll
  for (int t = 0; t < 8; ++t) PIN2(acc[0][t], acc[1][t]);
  bf16x8 wf[2][8];
#define DFR(u, M, t) do { if ((M) == 1) wf[(u) & 1][t] = ldtr2(a[(t) & 3] + (u) * 16384 + ((t) >> 2) * 8192, a[(t) & 3] + (u) * 16384 + ((t) >> 2) * 8192 + 2048); \
    else if ((M) == 2) wf[(u) & 1][t] = ldtr2(a2l[(t) & 3] + (u) * 16384 + ((t) >> 2) * 4096, a2l[(t) & 3] + (u) * 16384 + ((t) >> 2) * 4096 + 2048); } while (0)
#define DMM(u, CM, NM, t) do { DFR(((u) + 1) & 3, NM, t); SBAR(); constexpr int c_ = (CM) == 1 ? (t) : ((t) & 3), h_ = (CM) == 1 ? 0 : 2 * ((t) >> 2); \
    acc[0][c_] = MFMA16(wf[(u) & 1][t], __builtin_bit_cast(bf16x8, ar[u][h_]), acc[0][c_]); acc[1][c_] = MFMA16(wf[(u) & 1][t], __builtin_bit_cast(bf16x8, ar[u][h_ + 1]), acc[1][c_]); \
    PIN2(acc[0][c_], acc[1][c_]); SBAR(); } while (0)
#define DSTEP(u, N, CM, NM, PRE, L0, L1, L2, L3, L4, L5, L6, L7) do { \
    if ((CM) == 1) asm volatile("s_waitcnt vmcnt(" #N ") lgkmcnt(0)\n\ts_barrier" : "+v"(ar[u][0]), "+v"(ar[u][1]) :: "memory"); \
    else asm volatile("s_waitcnt vmcnt(" #N ") lgkmcnt(0)\n\ts_barrier" : "+v"(ar[u][0]), "+v"(ar[u][1]), "+v"(ar[u][2]), "+v"(ar[u][3]) :: "memory"); \
    PRE; SBAR(); \
    DMM(u, CM, NM, 0); L0; DMM(u, CM, NM, 1); L1; DMM(u, CM, NM, 2); L2; DMM(u, CM, NM, 3); L3; \
    DMM(u, CM, NM, 4); L4; DMM(u, CM, NM, 5); L5; DMM(u, CM, NM, 6); L6; DMM(u, CM, NM, 7); L7; } while (0)
#define D1STD(u) do { D1NEXT(); DSTEP(u, 8, 1, 1, (void)0, D1W(u, 0), (void)0, D1W(u, 1), (void)0, D1A(((u) + 3) & 3, 0), (void)0, D1A(((u) + 3) & 3, 1), (void)0); } while (0)
  asm volatile("s_waitcnt vmcnt(12)\n\ts_barrier" ::: "memory");
#pragma unroll
  for (int t = 0; t < 8; ++t) DFR(0, 1, t);
  lds_ptr xb = (lds_ptr)lds + 65536;
  f32x4 snd[4]; u32x4 rcv[4], xr[2]; unsigned fl;
  const int g = lane >> 4, tok = tm * 128 + wm * 32 + wk * 16 + (lane & 15);
  char* pmine = (char*)part + (size_t)(blockIdx.x * 8 + w) * 4096; const char* ptheir = (const char*)part + (size_t)((blockIdx.x ^ 1) * 8 + w) * 4096;
  unsigned* fmine = flag + (blockIdx.x * 8 + w) * 32; const unsigned* ftheir = flag + ((blockIdx.x ^ 1) * 8 + w) * 32;
#define DSEND_WRITE() do { _Pragma("unroll") for (int t = 0; t < 4; ++t) *(LAS f32x4*)(xb + w * 4096 + t * 1024 + lane * 16) = wk ? acc[0][4 + t] : acc[1][4 + t]; } while (0)
#define DSEND_READ() do { _Pragma("unroll") for (int t = 0; t < 4; ++t) snd[t] = *(const LAS f32x4*)(xb + (w ^ 4) * 4096 + t * 1024 + lane * 16) + (wk ? acc[1][4 + t] : acc[0][4 + t]); } while (0)
#define DSEND(k) asm volatile("global_store_dwordx4 %0, %1, %2 offset:%3 sc1\n\ts_nop 1" :: "v"(vA), "v"(snd[k]), "s"(pmine), "n"((k) * 1024) : "memory")
#define DFLAG_ST() asm volatile("global_store_dword %0, %1, %2 sc1" :: "v"(0u), "v"(1u), "s"(fmine) : "memory")
#define DFLAG_LD() asm volatile("global_load_dword %0, %1, %2 sc1" : "=v"(fl) : "v"(0u), "s"(ftheir) : "memory")
#define DRECV(k) asm volatile("global_load_dwordx4 %0, %1, %2 offset:%3 sc1" : "=v"(rcv[k]) : "v"(vA), "s"(ptheir), "n"((k) * 1024) : "memory")
#define DEL(k) do { if ((k) < 2) GLOAD4(xr[(k) & 1], (const char*)x1fm + (size_t)((tn * 2 + (k)) * 128 + tm * 8 + wm * 2 + wk) * 1024, vA, 0); \
    else glds16(modf + 5120 + tn * 64, (unsigned)((lane & 15) * 16), lds0 + 98304 + w * 1024); } while (0)
#pragma unroll 1
  for (int s = 0; s < 16; s += 4) { D1STD(0); D1STD(1); D1STD(2); D1STD(3); }
  ST(4);
  D1NEXT(); DSTEP(0, 8, 1, 1, (void)0, D2W(0, 0, 0), (void)0, D2W(0, 0, 1), (void)0, D1A(3, 0), (void)0, D1A(3, 1), (void)0);
  DSTEP(1, 8, 1, 1, (void)0, D2W(1, 1, 0), D2W(1, 1, 1), D2A(0, 0, 0), (void)0, D2A(0, 0, 1), D2A(0, 0, 2), D2A(0, 0, 3), (void)0);
  DSTEP(2, 10, 1, 1, (void)0, D2W(2, 2, 0), D2W(2, 2, 1), D2A(1, 1, 0), (void)0, D2A(1, 1, 1), D2A(1, 1, 2), D2A(1, 1, 3), (void)0);
  DSTEP(3, 12, 1, 2, (void)0, D2W(3, 3, 0), D2W(3, 3, 1), D2A(2, 2, 0), (void)0, D2A(2, 2, 1), D2A(2, 2, 2), D2A(2, 2, 3), (void)0);
  ST(5);
  DSEND_WRITE();
  DSTEP(0, 12, 2, 2, DSEND_READ(), D2W(0, 4, 0), D2W(0, 4, 1), D2A(3, 3, 0), D2A(3, 3, 1), D2A(3, 3, 2), D2A(3, 3, 3), DSEND(0), DSEND(1));
  DSTEP(1, 14, 2, 2, (void)0, D2W(1, 5, 0), D2W(1, 5, 1), D2A(0, 4, 0), D2A(0, 4, 1), D2A(0, 4, 2), D2A(0, 4, 3), DSEND(2), DSEND(3));
  DSTEP(2, 16, 2, 2, (void)0, D2W(2, 6, 0), D2W(2, 6, 1), D2A(1, 5, 0), (void)0, D2A(1, 5, 1), D2A(1, 5, 2), D2A(1, 5, 3), (void)0);
  DSTEP(3, 16, 2, 2, (void)0, D2W(3, 7, 0), D2W(3, 7, 1), D2A(2, 6, 0), (void)0, D2A(2, 6, 1), D2A(2, 6, 2), D2A(2, 6, 3), (void)0);
  DSTEP(0, 12, 2, 2, (void)0, DFLAG_ST(), D2A(3, 7, 0), (void)0, D2A(3, 7, 1), D2A(3, 7, 2), (void)0, D2A(3, 7, 3), (void)0);
  DSTEP(1, 11, 2, 2, (void)0, DEL(0), (void)0, DEL(1), (void)0, (void)0, DEL(2), (void)0, (void)0);
  DSTEP(2, 8, 2, 2, (void)0, DFLAG_LD(), (void)0, (void)0, (void)0, (void)0, (void)0, (void)0, (void)0);
  DSTEP(3, 0, 2, 0, (void)0, DRECV(0), DRECV(1), DRECV(2), DRECV(3), (void)0, (void)0, (void)0, (void)0);
  ST(2);
#pragma unroll
  for (int t = 0; t < 4; ++t) *(LAS f32x4*)(xb + w * 4096 + t * 1024 + lane * 16) = wk ? acc[0][t] : acc[1][t];
  asm volatile("s_waitcnt vmcnt(0) lgkmcnt(0)\n\ts_barrier" : "+v"(xr[0]), "+v"(xr[1]), "+v"(rcv[0]), "+v"(rcv[1]), "+v"(rcv[2]), "+v"(rcv[3]), "+v"(fl) :: "memory");
  if (RFL(fl) == 0u) {
    for (int it = 0; it < (1 << 22); ++it) { if (RFL(__hip_atomic_load(ftheir, __ATOMIC_RELAXED, __HIP_MEMORY_SCOPE_AGENT)) != 0u) break; __builtin_amdgcn_s_sleep(2); }
    asm volatile("global_load_dwordx4 %0, %4, %5 sc1\n\tglobal_load_dwordx4 %1, %4, %5 offset:1024 sc1\n\tglobal_load_dwordx4 %2, %4, %5 offset:2048 sc1\n\t"
                 "global_load_dwordx4 %3, %4, %5 offset:3072 sc1\n\ts_waitcnt vmcnt(0)" : "=&v"(rcv[0]), "=&v"(rcv[1]), "=&v"(rcv[2]), "=&v"(rcv[3]) : "v"(vA), "s"(ptheir) : "memory");
  }
#pragma unroll
  for (int jj = 0; jj < 2; ++jj) {
    unsigned pk[2][2];
#pragma unroll
    for (int tt = 0; tt < 2; ++tt) { const int t = 2 * jj + tt;
      const f32x4 o = (*(const LAS f32x4*)(xb + (w ^ 4) * 4096 + t * 1024 + lane * 16) + (wk ? acc[1][t] : acc[0][t])) + __builtin_bit_cast(f32x4, rcv[t]);
      const f32x4 gb = *(const LAS f32x4*)(L + 98304 + w * 1024 + t * 64 + g * 16);
      pk[tt][0] = cvtpk(bflo(xr[jj][2 * tt]) + gb[0] * o[0], bfhi(xr[jj][2 * tt]) + gb[1] * o[1]);
      pk[tt][1] = cvtpk(bflo(xr[jj][2 * tt + 1]) + gb[2] * o[2], bfhi(xr[jj][2 * tt + 1]) + gb[3] * o[3]); }
    auto r0 = __builtin_amdgcn_permlane16_swap(pk[0][0], pk[1][0], false, false);
    auto r1 = __builtin_amdgcn_permlane16_swap(pk[0][1], pk[1][1], false, false);
    *(u32x4*)(out + (size_t)tok * D_ + tn * 64 + jj * 32 + (g & 1) * 16 + (g >> 1) * 8) = (u32x4){r0[0], r1[0], r0[1], r1[1]};
  }
  ST(6);
  ST_END(3);
}

extern "C" __global__ void k7_end() {}

extern "C" void kernel_launch(void* const* d_in, const int* in_sizes, int n_in, void* d_out, int out_size, void* d_ws, size_t ws_size, hipStream_t stream) {
  const bf16_t* x = (const bf16_t*)d_in[0]; const bf16_t* wq = (const bf16_t*)d_in[1]; const bf16_t* wk = (const bf16_t*)d_in[2];
  const bf16_t* wv = (const bf16_t*)d_in[3]; const bf16_t* wo = (const bf16_t*)d_in[4]; const bf16_t* wg = (const bf16_t*)d_in[6];
  const bf16_t* wu = (const bf16_t*)d_in[7]; const bf16_t* wd = (const bf16_t*)d_in[8]; const bf16_t* c = (const bf16_t*)d_in[9];
  const bf16_t* wmod = (const bf16_t*)d_in[10];
  char* ws = (char*)d_ws;
  float* modp = (float*)(ws + WS_MODP); float* modf = (float*)(ws + WS_MODF); bf16_t* cvec = (bf16_t*)(ws + WS_CVEC);
  bf16_t* wkvf = (bf16_t*)(ws + WS_WKVF); bf16_t* kvf = (bf16_t*)(ws + WS_KVF);
  float* utp = (float*)(ws + WS_UTP); float* utf = (float*)(ws + WS_UTF); unsigned* flag = (unsigned*)(ws + WS_FLAG); float* part = (float*)(ws + WS_PART); f32x2* stat = (f32x2*)(ws + WS_STAT);
  bf16_t* hfm = (bf16_t*)(ws + WS_H); bf16_t* ofm = (bf16_t*)(ws + WS_O); bf16_t* x1fm = (bf16_t*)(ws + WS_X1); bf16_t* yfm = (bf16_t*)(ws + WS_Y); bf16_t* actfm = (bf16_t*)(ws + WS_ACT);
  (void)hipFuncSetAttribute((const void*)k3_attn, hipFuncAttributeMaxDynamicSharedMemorySize, 163840);
  (void)hipFuncSetAttribute((const void*)k4_oproj, hipFuncAttributeMaxDynamicSharedMemorySize, 163840);
  (void)hipFuncSetAttribute((const void*)k5_gateup, hipFuncAttributeMaxDynamicSharedMemorySize, 139264);
  (void)hipFuncSetAttribute((const void*)k6_down, hipFuncAttributeMaxDynamicSharedMemorySize, 106496);
  k1_mod<<<288, 256, 0, stream>>>(c, wmod, wk, wv, modp, wkvf, x STAMP_PASS(0));
  k2_ln_kv<<<256, 512, 0, stream>>>(x, modp, wkvf, wq, hfm, kvf, modf, cvec, c, wmod STAMP_PASS(1));
  k3_attn<<<256, 512, 163840, stream>>>(hfm, wq, kvf, ofm, wg, wu, cvec, utp, wo, wd, x, modp, modf STAMP_PASS(2));
  k4_oproj<<<256, 512, 163840, stream>>>(ofm, wo, x, modf, x1fm, yfm, stat, utp, utf, flag STAMP_PASS(3));
  k5_gateup<<<224, 512, 139264, stream>>>(yfm, wg, wu, stat, utf, actfm STAMP_PASS(4));
  k6_down<<<256, 512, 106496, stream>>>(actfm, wd, x1fm, modf, (bf16_t*)d_out, flag, part STAMP_PASS(5));
}
```

```cpp
#include <hip/hip_runtime.h>
#include <stdint.h>

typedef unsigned short bf16_t;
typedef short bf16x8 __attribute__((ext_vector_type(8)));
typedef short s16x4 __attribute__((ext_vector_type(4)));
typedef float f32x2 __attribute__((ext_vector_type(2)));
typedef float f32x4 __attribute__((ext_vector_type(4)));
typedef float f32x16 __attribute__((ext_vector_type(16)));
typedef unsigned u32x2 __attribute__((ext_vector_type(2)));
typedef unsigned u32x4 __attribute__((ext_vector_type(4)));
typedef __bf16 bf16x2_t __attribute__((ext_vector_type(2)));
#define LAS __attribute__((address_space(3)))
typedef LAS const char* lds_cptr;
typedef LAS char* lds_ptr;
#define DI __device__ __forceinline__

constexpr int S_ = 2048, D_ = 1024, F_ = 3584, NMOD = 6144;
constexpr float LN_EPS = 1e-5f;
constexpr float QSCALE = 0.17677669529663687f * 1.4426950408889634f;

constexpr size_t WS_MODP = 0;
constexpr size_t WS_GBF  = 160 * 1024;
constexpr size_t WS_RS   = 192 * 1024;
constexpr size_t WS_KVF  = 384 * 1024;
constexpr size_t WS_CNT  = 33u << 20;
constexpr int C_H = 0, C_O = 1024, C_Y = 1280, C_ACT = 1536, C_SIDE = 1792, C_SB = 1824, C_RS = 1856, C_GB = 2048, C_KV = 2080, C_RT = 2088, C_EPOCH = 2100, C_PART = 2112;
constexpr size_t WS_STAT = 768 * 1024;
constexpr size_t WS_O    = 5u << 20;
constexpr size_t WS_H    = 9u << 20;
constexpr size_t WS_Y    = 13u << 20;
constexpr size_t WS_ACT  = 17u << 20;
constexpr size_t WS_UTP  = 32u << 20;
constexpr size_t WS_PART = 42u << 20;
constexpr size_t WS_WDT  = 34u << 20;

DI unsigned cvtpk(float lo, float hi) { f32x2 v = {lo, hi}; bf16x2_t b = __builtin_convertvector(v, bf16x2_t); return __builtin_bit_cast(unsigned, b); }
DI float bflo(unsigned u) { return __uint_as_float(u << 16); }
DI float bfhi(unsigned u) { return __uint_as_float(u & 0xffff0000u); }
DI float rbf(float x) { return bflo(cvtpk(x, 0.f) & 0xffffu); }
DI float bf2f(bf16_t b) { return __uint_as_float((unsigned)b << 16); }
DI void glds16(const void* sbase, unsigned voff, unsigned lds_dst) {
  unsigned keep;
  asm volatile("s_mov_b32 %0, m0\n\ts_mov_b32 m0, %3\n\ts_nop 2\n\tglobal_load_lds_dwordx4 %1, %2\n\ts_mov_b32 m0, %0"
               : "=&s"(keep) : "v"(voff), "s"(sbase), "s"(lds_dst) : "memory");
}
DI void glds16c(const void* sbase, unsigned voff, unsigned lds_dst) {
  unsigned keep;
  asm volatile("s_mov_b32 %0, m0\n\ts_mov_b32 m0, %3\n\ts_nop 2\n\tglobal_load_lds_dwordx4 %1, %2 sc1\n\ts_mov_b32 m0, %0"
               : "=&s"(keep) : "v"(voff), "s"(sbase), "s"(lds_dst) : "memory");
}
#define GLOAD4C(dst, sbase, voff) asm volatile("s_nop 4\n\tglobal_load_dwordx4 %0, %1, %2 sc1" : "=v"(dst) : "v"(voff), "s"(sbase) : "memory")
#define GLOAD4(dst, sbase, voff, imm) asm volatile("s_nop 4\n\tglobal_load_dwordx4 %0, %1, %2 offset:%3" : "=v"(dst) : "v"(voff), "s"(sbase), "n"(imm) : "memory")
#define GLOAD4NT(dst, sbase, voff) asm volatile("s_nop 4\n\tglobal_load_dwordx4 %0, %1, %2 nt" : "=v"(dst) : "v"(voff), "s"(sbase) : "memory")
#define GLOAD1(dst, sbase, voff) asm volatile("s_nop 4\n\tglobal_load_dword %0, %1, %2" : "=v"(dst) : "v"(voff), "s"(sbase) : "memory")
#define GLOADH(dst, sbase, voff) asm volatile("s_nop 4\n\tglobal_load_ushort %0, %1, %2" : "=v"(dst) : "v"(voff), "s"(sbase) : "memory")
typedef short v4i16_t __attribute__((ext_vector_type(4)));
DI s16x4 ldtr(lds_cptr p) { return __builtin_bit_cast(s16x4, __builtin_amdgcn_ds_read_tr16_b64_v4i16((LAS v4i16_t*)p)); }
DI bf16x8 ldtr2(lds_cptr p0, lds_cptr p1) { s16x4 a = ldtr(p0), b = ldtr(p1); return (bf16x8){a[0], a[1], a[2], a[3], b[0], b[1], b[2], b[3]}; }
DI bf16x8 ld128(lds_cptr p) { return *(const LAS bf16x8*)p; }
#define MFMA32(a, b, c) __builtin_amdgcn_mfma_f32_32x32x16_bf16(a, b, c, 0, 0, 0)
#define MFMA16(a, b, c) __builtin_amdgcn_mfma_f32_16x16x32_bf16(a, b, c, 0, 0, 0)
#define SBAR() __builtin_amdgcn_sched_barrier(0)
#define BAR() asm volatile("s_barrier" ::: "memory")
#define WAITV(n) asm volatile("s_waitcnt vmcnt(" #n ")" ::: "memory")
#define WAITL() asm volatile("s_waitcnt lgkmcnt(0)" ::: "memory")
#define RFL(x) __builtin_amdgcn_readfirstlane(x)
#define PIN(x) asm volatile("" : "+v"(x))
#define PIN2(x, y) asm volatile("" : "+v"(x), "+v"(y))
#define WT 1
DI void store16(void* p, u32x4 v) {
  asm volatile("global_store_dwordx4 %0, %1, off sc1\n\ts_nop 1" :: "v"(p), "v"(v) : "memory");
}
DI float sum4g(float x) {
  auto a = __builtin_amdgcn_permlane16_swap(__float_as_uint(x), __float_as_uint(x), false, false); x = __uint_as_float(a[0]) + __uint_as_float(a[1]);
  auto b = __builtin_amdgcn_permlane32_swap(__float_as_uint(x), __float_as_uint(x), false, false); return __uint_as_float(b[0]) + __uint_as_float(b[1]);
}
template <int CTRL> DI float dpp_add(float x) { return x + __int_as_float(__builtin_amdgcn_update_dpp(0, __float_as_int(x), CTRL, 0xf, 0xf, true)); }
DI float sum64(float x) { x = dpp_add<0xB1>(x); x = dpp_add<0x4E>(x); x = dpp_add<0x141>(x); x = dpp_add<0x140>(x); return sum4g(x); }
#define KEEP_S(p) asm volatile("" :: "s"(p))
#define TOUCH(dummy, sbase, voff) asm volatile("s_nop 4\n\tglobal_load_dword %0, %1, %2" : "+v"(dummy) : "v"(voff), "s"(sbase) : "memory")
#define TOUCH255(sbase, voff) asm volatile("s_nop 4\n\tglobal_load_dword v255, %0, %1" :: "v"(voff), "s"(sbase) : "memory", "v255")
#define CODEPF 1
#define SYM_ADDR(dst, name) asm volatile("s_getpc_b64 vcc\n\ts_add_u32 vcc_lo, vcc_lo, " #name "@rel32@lo+4\n\ts_addc_u32 vcc_hi, vcc_hi, " #name "@rel32@hi+12\n\ts_mov_b64 %0, vcc" : "=s"(dst) :: "vcc", "scc")
#define CODE_OFF(a, b, i, lane) ({ const int len_ = (int)((b) - (a)) - 4, o_ = (i) * 8192 + (lane) * 128; (unsigned)(o_ < len_ ? o_ : (len_ > 0 ? len_ : 0)); })
typedef __bf16 bf16x8n __attribute__((ext_vector_type(8)));
typedef __bf16 bf16x16n __attribute__((ext_vector_type(16)));
typedef unsigned u32x8 __attribute__((ext_vector_type(8)));

#define STAMP_ARG
#define STAMP_PASS(k)
#define ST_DECL
#define STW(i)
#define ST(i)
#define ST_END(i)

DI float silu_bf(float cv) { return rbf(cv * __builtin_amdgcn_rcpf(1.f + __expf(-cv))); }
DI f32x2 up2(unsigned u) { return (f32x2){bflo(u), bfhi(u)}; }
DI unsigned wimg_src_chunk(int lane) { return (unsigned)(((lane & 7) ^ (((lane >> 4) & 3) << 1)) << 4); }
DI int wimg_rd(int lane, int t) { const int g = lane >> 4, q = (lane & 15) >> 2, r8 = 4 * (g & 1) + q; return (g >> 1) * 1024 + r8 * 128 + ((t ^ (r8 >> 1)) * 32) + (lane & 3) * 8; }

DI bf16x8 pack8(const f32x16& p, int s) {
  u32x4 r = {cvtpk(p[8 * s], p[8 * s + 1]), cvtpk(p[8 * s + 2], p[8 * s + 3]), cvtpk(p[8 * s + 4], p[8 * s + 5]), cvtpk(p[8 * s + 6], p[8 * s + 7])};
  return __builtin_bit_cast(bf16x8, r);
}
DI float rowmax16(const f32x16& p) {
  float a = fmaxf(fmaxf(p[0], p[1]), p[2]), b = fmaxf(fmaxf(p[3], p[4]), p[5]);
  a = fmaxf(fmaxf(a, p[6]), p[7]); b = fmaxf(fmaxf(b, p[8]), p[9]); a = fmaxf(fmaxf(a, p[10]), p[11]); b = fmaxf(fmaxf(b, p[12]), p[13]);
  a = fmaxf(fmaxf(a, p[14]), p[15]); a = fmaxf(a, b);
  auto rr = __builtin_amdgcn_permlane32_swap(__float_as_uint(a), __float_as_uint(a), false, false);
  return fmaxf(__uint_as_float(rr[0]), __uint_as_float(rr[1]));
}
DI bf16x8 plo(const u32x8& p) { return __builtin_bit_cast(bf16x8, __builtin_shufflevector(p, p, 0, 1, 2, 3)); }
DI bf16x8 phi(const u32x8& p) { return __builtin_bit_cast(bf16x8, __builtin_shufflevector(p, p, 4, 5, 6, 7)); }
#define ROWSUM(sel, p, acc, idx) __builtin_amdgcn_smfmac_f32_16x16x64_bf16(__builtin_bit_cast(bf16x8n, sel), __builtin_bit_cast(bf16x16n, p), acc, idx, 0, 0)

DI void raise(unsigned* f, unsigned tag) { __hip_atomic_store(f, tag, __ATOMIC_RELAXED, __HIP_MEMORY_SCOPE_AGENT); }
#define GSL 2
#define PSL 2
template <int SLEEP> DI void await(const unsigned* p, unsigned tag) {
#pragma unroll 1
  for (int it = 0; it < (1 << 20); ++it) { if (__all(__hip_atomic_load(p, __ATOMIC_RELAXED, __HIP_MEMORY_SCOPE_AGENT) == tag)) break; __builtin_amdgcn_s_sleep(SLEEP <= 2 ? PSL : SLEEP); }
}
template <int SLEEP> DI void await_n(const unsigned* f, int n, unsigned tag) { const int l = threadIdx.x & 63; await<SLEEP>(f + (l < n ? l : 0) * 32, tag); }
DI bool all4(u32x4 v, unsigned tag) { return __all(v[0] == tag && v[1] == tag && v[2] == tag && v[3] == tag); }
DI void await_packed(const unsigned* f, unsigned tag) {
#pragma unroll 1
  for (int it = 0; it < (1 << 18); ++it) { u32x4 v;
    asm volatile("s_nop 4\n\tglobal_load_dwordx4 %0, %1, %2 sc1\n\ts_waitcnt vmcnt(0)" : "=&v"(v) : "v"((threadIdx.x & 63) * 16), "s"(f) : "memory");
    if (all4(v, tag)) break; __builtin_amdgcn_s_sleep(16); }
}
DI f32x4 rbf4(f32x4 a) { const unsigned p = cvtpk(a[0], a[1]), q = cvtpk(a[2], a[3]); return (f32x4){bflo(p), bfhi(p), bflo(q), bfhi(q)}; }
DI float fold8(const f32x2 (&a)[4], int lane) {
  float b[4], c[2];
#pragma unroll
  for (int j = 0; j < 4; ++j) { auto r = __builtin_amdgcn_permlane32_swap(__float_as_uint(a[j >> 1][j & 1]), __float_as_uint(a[2 + (j >> 1)][j & 1]), false, false); b[j] = __uint_as_float(r[0]) + __uint_as_float(r[1]); }
#pragma unroll
  for (int j = 0; j < 2; ++j) { auto r = __builtin_amdgcn_permlane16_swap(__float_as_uint(b[j]), __float_as_uint(b[2 + j]), false, false); c[j] = __uint_as_float(r[0]) + __uint_as_float(r[1]); }
  c[0] = dpp_add<0x128>(c[0]); c[1] = dpp_add<0x128>(c[1]);
  return (lane & 8) ? c[1] : c[0];
}
DI f32x4 gather_mod(const char* modp, unsigned v0, unsigned v1, unsigned tag) {
  u32x4 a[8];
#pragma unroll 1
  for (int it = 0; it < (1 << 18); ++it) {
    asm volatile("s_nop 4\n\tglobal_load_dwordx4 %0, %8, %10 sc1\n\tglobal_load_dwordx4 %1, %9, %10 sc1\n\tglobal_load_dwordx4 %2, %8, %11 sc1\n\tglobal_load_dwordx4 %3, %9, %11 sc1\n\t"
                 "global_load_dwordx4 %4, %8, %12 sc1\n\tglobal_load_dwordx4 %5, %9, %12 sc1\n\tglobal_load_dwordx4 %6, %8, %13 sc1\n\tglobal_load_dwordx4 %7, %9, %13 sc1\n\ts_waitcnt vmcnt(0)"
                 : "=&v"(a[0]), "=&v"(a[1]), "=&v"(a[2]), "=&v"(a[3]), "=&v"(a[4]), "=&v"(a[5]), "=&v"(a[6]), "=&v"(a[7])
                 : "v"(v0), "v"(v1), "s"(modp), "s"(modp + 32768), "s"(modp + 65536), "s"(modp + 98304) : "memory");
    unsigned bad = 0;
#pragma unroll
    for (int i = 0; i < 8; ++i) bad |= (a[i][1] ^ tag) | (a[i][3] ^ tag);
    if (__all(bad == 0u)) break;
    __builtin_amdgcn_s_sleep(GSL);
  }
  f32x4 r;
#pragma unroll
  for (int j = 0; j < 4; ++j) r[j] = (__uint_as_float(a[j >> 1][(j & 1) * 2]) + __uint_as_float(a[2 + (j >> 1)][(j & 1) * 2])) + (__uint_as_float(a[4 + (j >> 1)][(j & 1) * 2]) + __uint_as_float(a[6 + (j >> 1)][(j & 1) * 2]));
  return rbf4(r);
}
DI void glds16f(const void* sbase, unsigned voff, unsigned lds_dst) { asm volatile("s_mov_b32 m0, %2\n\ts_nop 0\n\tglobal_load_lds_dwordx4 %0, %1 sc1" :: "v"(voff), "s"(sbase), "s"(lds_dst) : "memory"); }
#define XNT 1
DI void glds16x(const void* sbase, unsigned voff, unsigned lds_dst) {
  if (XNT) asm volatile("s_mov_b32 m0, %2\n\ts_nop 3\n\tglobal_load_lds_dwordx4 %0, %1 nt" :: "v"(voff), "s"(sbase), "s"(lds_dst) : "memory"); else glds16(sbase, voff, lds_dst); }
#define BNT 0
DI void glds16w(const void* sbase, unsigned voff, unsigned lds_dst) {
  if (BNT) asm volatile("s_mov_b32 m0, %2\n\ts_nop 0\n\tglobal_load_lds_dwordx4 %0, %1 nt" :: "v"(voff), "s"(sbase), "s"(lds_dst) : "memory");
  else asm volatile("s_mov_b32 m0, %2\n\ts_nop 0\n\tglobal_load_lds_dwordx4 %0, %1" :: "v"(voff), "s"(sbase), "s"(lds_dst) : "memory"); }
#define MFMA16A(acc, a, b) asm volatile("v_mfma_f32_16x16x32_bf16 %0, %1, %2, %0" : "+v"(acc) : "v"(a), "v"(b))
#define GUPF 1
#define EARLYB 2
#define IDS() int tid = threadIdx.x; PIN(tid); const int lane = tid & 63; const unsigned vlin = (unsigned)(lane * 16)

extern "C" __global__ __launch_bounds__(512, 2) void k3_main(const bf16_t* x, const bf16_t* wq, const bf16_t* wkp, const bf16_t* wvp, const bf16_t* wo, const bf16_t* wg, const bf16_t* wu, const bf16_t* wd,
                                                             const bf16_t* cnd, const bf16_t* wmod, char* ws, bf16_t* out STAMP_ARG) {
  extern __shared__ __attribute__((aligned(16))) char lds[];
  STW(0);
#define EARLYPF 1
  if (EARLYPF == 1 && blockIdx.x < 16) { const char *c0, *c1; SYM_ADDR(c0, k3_main); SYM_ADDR(c1, k7_end); TOUCH255(c0, CODE_OFF(c0, c1, 0, threadIdx.x)); }
  if (EARLYPF == 2 && (blockIdx.x & 32) && RFL(threadIdx.x >> 6) == 0) { const char *c0, *c1; SYM_ADDR(c0, k3_main); SYM_ADDR(c1, k7_end);
    TOUCH255(c0, CODE_OFF(c0, c1, 0, (((blockIdx.x >> 6) * 4 + ((blockIdx.x >> 3) & 3)) * 32 + (threadIdx.x & 31)))); }
  KEEP_S(x); KEEP_S(wq); KEEP_S(wkp); KEEP_S(wvp); KEEP_S(wo); KEEP_S(wg); KEEP_S(wu); KEEP_S(wd); KEEP_S(cnd); KEEP_S(wmod); KEEP_S(ws); KEEP_S(out);
  char* hfm = ws + WS_H; char* kvf = ws + WS_KVF; char* ofm = ws + WS_O; char* utp = ws + WS_UTP; char* modp = ws + WS_MODP;
  char* wdt = ws + WS_WDT; char* yfm = ws + WS_Y; char* stat = ws + WS_STAT; char* actfm = ws + WS_ACT; unsigned* cnt = (unsigned*)(ws + WS_CNT);
  unsigned tag;
  const int w = RFL(threadIdx.x >> 6), wm = w & 3, wk = w >> 2;
#define MAP2 0
  const int xcd = blockIdx.x & 7, bi = blockIdx.x >> 3;
  const int tm = MAP2 ? xcd * 2 + (bi & 1) : (xcd >> 1) * 4 + (bi & 3), tn = MAP2 ? bi >> 1 : (xcd & 1) * 8 + (bi >> 2);
  const unsigned lds0 = (unsigned)(uintptr_t)lds;
  const lds_cptr L = (lds_cptr)lds;
  constexpr int R0 = 0, R1 = 32768, R2 = 65536, R3 = 98304, R4 = 131072;
#define OMASK 1
  const int orot = ((bi >> 2) & OMASK) * 2;
#define OWL(j, q, vo) glds16((const char*)wo + (size_t)((((q) >> 1) * 16 + (((j) * 4 + ((q) & 1) * 2) ^ orot)) * 32) * 2048, vo, \
    lds0 + ((j) == 0 ? R3 : (j) == 1 ? R4 : (j) == 2 ? R1 : R2) + ((q) * 8 + w) * 1024)
  {
    IDS();
    const int hi = lane >> 5;
#define QMASK 1
    const int crot = bi & 7, g = lane >> 4;
    const int sl2 = R2 + w * 4096;
    u32x4 hf[32];
    const char* hb = hfm + (size_t)(wk * 32 * 64 + tm * 4 + wm) * 1024;
#define HLOAD(i) GLOAD4C(hf[i], hb + (size_t)(i) * 65536, vlin)
#define F4(v) __builtin_bit_cast(f32x4, v)
#define B8(v) __builtin_bit_cast(bf16x8, v)
    {
      const int tt = tm * 8 + (tn >> 1), z = tn & 1, cg = blockIdx.x & 63, ks = blockIdx.x >> 6;
      const bool pub = wk == z;
      u32x4 xh[4]; f32x2 xv[16];
      {
        u32x4 wm4[4]; u32x2 cr; unsigned ev;
#define MLD() do { _Pragma("unroll") for (int i = 0; i < 4; ++i) GLOAD4NT(wm4[i], wmod + (size_t)(ks * 256 + i) * NMOD + cg * 64 + (cg >= 48 ? 1024 : 0), (unsigned)((tid >> 3) * (NMOD * 8) + (tid & 7) * 16)); } while (0)
#define XLD() do { _Pragma("unroll") for (int i = 0; i < 4; ++i) glds16x((const char*)x + (size_t)(tt * 16 + 4 * i) * 2048 + w * 256, (unsigned)(g * 2048 + (((lane & 15) ^ (4 * i + g)) << 4)), lds0 + sl2 + i * 1024); } while (0)
#define QNT 0
#define TOUCHQ(sbase, voff) do { if (QNT) asm volatile("s_nop 4\n\tglobal_load_dword v255, %0, %1 nt" :: "v"(voff), "s"(sbase) : "memory", "v255"); else TOUCH255(sbase, voff); } while (0)
#define QTOUCH() do { const int r_ = ((bi >> 3) * 4 + (bi & 3)) * 32 + w * 4 + ((lane >> 3) & 3); TOUCH255(wq, (unsigned)(((r_ >> 6) * 128 + (z ^ 1) * 64 + (r_ & 63)) * 2048 + (xcd & 1) * 1024 + (lane & 7) * 128)); } while (0)
#define WPRE 8
#define FV 0
#define MSUM() do { f32x2 acc[4] = {}; \
        _Pragma("unroll") for (int i = 0; i < 4; ++i) { const float sv = silu_bf((i & 1) ? bfhi(cr[i >> 1]) : bflo(cr[i >> 1])); _Pragma("unroll") for (int j = 0; j < 4; ++j) acc[j] += up2(wm4[i][j]) * sv; } \
        *(LAS float*)((lds_ptr)lds + R0 + w * 256 + ((lane & 7) * 8 + (lane >> 3)) * 4) = fold8(acc, lane); } while (0)
#define MPUB() do { if (w == 0) { float s = 0.f; _Pragma("unroll") for (int ww = 0; ww < 8; ++ww) s += *(const LAS float*)(L + R0 + ww * 256 + lane * 4); \
          asm volatile("s_nop 4\n\tglobal_store_dwordx2 %0, %1, %2 sc1" :: "v"((unsigned)(lane * 8)), "v"((u32x2){__float_as_uint(s), tag}), "s"(modp + (ks * 4096 + cg * 64) * 8) : "memory"); } } while (0)
#define XSTAT() do { _Pragma("unroll") for (int jj = 0; jj < 4; ++jj) xh[jj] = *(const LAS u32x4*)(L + sl2 + (lane & 15) * 256 + (((4 * jj + g) ^ (lane & 15)) << 4)); \
        _Pragma("unroll") for (int j = 0; j < 16; ++j) xv[j] = up2(xh[j >> 2][j & 3]); \
        f32x2 s = (xv[0] + xv[1]) + (xv[2] + xv[3]); \
        _Pragma("unroll") for (int j = 4; j < 16; j += 4) s += (xv[j] + xv[j + 1]) + (xv[j + 2] + xv[j + 3]); \
        const float mw = sum4g(s[0] + s[1]) * (1.f / 128.f); f32x2 q0 = {0.f, 0.f}, q1 = {0.f, 0.f}; \
        _Pragma("unroll") for (int j = 0; j < 16; j += 2) { const f32x2 d0 = xv[j] - mw, d1 = xv[j + 1] - mw; q0 += d0 * d0; q1 += d1 * d1; } \
        q0 += q1; const float m2 = sum4g(q0[0] + q0[1]); \
        if (g == 0) *(LAS f32x2*)((lds_ptr)lds + sl2 + 3328 + lane * 8) = (f32x2){mw, m2}; } while (0)
#define MWAIT() asm volatile("s_waitcnt vmcnt(0)" : "+v"(wm4[0]), "+v"(wm4[1]), "+v"(wm4[2]), "+v"(wm4[3]), "+v"(cr), "+v"(ev) :: "memory")
#define MTAG() do { const unsigned epoch = RFL(ev); tag = (epoch ^ (epoch >> 15)) * 0x9E3779B1u + 0x7F4A7C15u; if (tag == epoch) tag ^= 0x5bd1e995u; } while (0)
#define CLD() do { asm volatile("s_nop 4\n\tglobal_load_dwordx2 %0, %1, %2" : "=v"(cr) : "v"((unsigned)((tid >> 3) * 8)), "s"(cnd + ks * 256) : "memory"); asm volatile("s_nop 4\n\tglobal_load_dword %0, %1, %2 sc1" : "=v"(ev) : "v"(0u), "s"(cnt + C_EPOCH * 32) : "memory"); } while (0)
        if (z == 0) {
          MLD(); CLD(); MWAIT(); XLD(); MTAG(); MSUM();
          asm volatile("s_waitcnt lgkmcnt(0)\n\ts_barrier" ::: "memory");
          MPUB(); if (FV & 1) QTOUCH();
          if (FV & 1) { if (w == 0) WAITV(2); else WAITV(1); } else { if (w == 0) WAITV(1); else WAITV(0); }
          XSTAT();
          asm volatile("s_waitcnt lgkmcnt(0)\n\ts_barrier" ::: "memory");
        } else {
          XLD();
          if (w == 7) TOUCH255((bi & 16) ? wvp : wkp, (unsigned)((((bi >> 3) & 1) * 4 + (bi & 3)) * 8192 + lane * 128));
          CLD(); WAITV(0);
          if (FV & 2) { MLD(); QTOUCH(); } else { QTOUCH(); MLD(); } XSTAT();
          MWAIT(); MTAG(); MSUM();
          asm volatile("s_waitcnt lgkmcnt(0)\n\ts_barrier" ::: "memory");
          MPUB();
        }
      }
      { const f32x2 p0 = *(const LAS f32x2*)(L + R2 + (2 * g) * 4096 + 3328 + (lane & 15) * 8), p1 = *(const LAS f32x2*)(L + R2 + (2 * g + 1) * 4096 + 3328 + (lane & 15) * 8);
        const float mean = sum4g(p0[0] + p1[0]) * 0.125f, d0 = p0[0] - mean, d1 = p1[0] - mean;
        const float rstd = __builtin_amdgcn_rsqf(sum4g((p0[1] + p1[1]) + 128.f * (d0 * d0 + d1 * d1)) * (1.f / D_) + LN_EPS);
#pragma unroll
        for (int j = 0; j < 16; ++j) { xv[j] = (xv[j] - mean) * rstd; PIN(xv[j]); } }
      if (pub) {
        const unsigned fo = (unsigned)((g >> 1) * 65536 + ((tt & 1) * 16 + (lane & 15) + 32 * (g & 1)) * 16);
#pragma unroll
        for (int jj = 0; jj < 4; ++jj) asm volatile("s_nop 4\n\tglobal_store_dwordx4 %0, %1, %2 sc1\n\ts_nop 1" :: "v"(fo),
          "v"((u32x4){cvtpk(xv[4 * jj][0], xv[4 * jj][1]), cvtpk(xv[4 * jj + 1][0], xv[4 * jj + 1][1]), cvtpk(xv[4 * jj + 2][0], xv[4 * jj + 2][1]), cvtpk(xv[4 * jj + 3][0], xv[4 * jj + 3][1])}),
          "s"(hfm + (size_t)((8 * w + 2 * jj) * 64 + (tt >> 1)) * 1024) : "memory");
      }
      const int wbase = wk * R3 + wm * 16384;
      u32x4 wq4[16];
      const unsigned wvoff = (unsigned)((lane >> 3) * 2048 + (lane & 7) * 16);
#define WQL(e) GLOAD4(wq4[e], (const char*)wq + (size_t)(w * 128 + (e) * 8) * 2048 + tn * 128, wvoff, 0)
#pragma unroll
      for (int i = 0; i < 8; ++i) glds16((const char*)(z ? wvp : wkp) + w * 8192 + i * 1024, vlin, lds0 + wbase + 8192 + i * 1024);
      if (pub) { WAITV(8); if (lane == 0) raise(cnt + (C_H + tm * 64 + tn * 4 + wm) * 32, tag); }
#pragma unroll
      for (int e = 0; e < WPRE; ++e) WQL(e);
      { const f32x4 m = rbf4(gather_mod(modp, (unsigned)(w * 1024) + vlin, (unsigned)(8192 + w * 1024) + vlin, tag) + (f32x4){0.f, 0.f, 1.f, 1.f});
        *(LAS f32x2*)((lds_ptr)lds + sl2 + 2048 + lane * 8) = (f32x2){m[0], m[1]}; *(LAS f32x2*)((lds_ptr)lds + sl2 + 2560 + lane * 8) = (f32x2){m[2], m[3]}; }
      if (w == 4 * z) await_n<2>(cnt + (C_H + tm * 64) * 32, 64, tag);
#pragma unroll
      for (int e = WPRE; e < 16; ++e) WQL(e);
      WAITL();
#pragma unroll
      for (int jj = 0; jj < 4; ++jj) { const lds_cptr p = L + sl2 + 2048 + jj * 128 + g * 32;
        const f32x4 c0 = *(const LAS f32x4*)(p + 512), c1 = *(const LAS f32x4*)(p + 512 + 16), s0 = *(const LAS f32x4*)(p), s1 = *(const LAS f32x4*)(p + 16);
        const f32x2 a = xv[4 * jj] * (f32x2){c0[0], c0[1]} + (f32x2){s0[0], s0[1]}, b = xv[4 * jj + 1] * (f32x2){c0[2], c0[3]} + (f32x2){s0[2], s0[3]};
        const f32x2 c = xv[4 * jj + 2] * (f32x2){c1[0], c1[1]} + (f32x2){s1[0], s1[1]}, d = xv[4 * jj + 3] * (f32x2){c1[2], c1[3]} + (f32x2){s1[2], s1[3]};
        xh[jj] = (u32x4){cvtpk(a[0], a[1]), cvtpk(b[0], b[1]), cvtpk(c[0], c[1]), cvtpk(d[0], d[1])}; }
      { f32x4 acc[2] = {};
        const lds_cptr wr = L + wbase + 8192 + (8 * g + ((lane & 15) >> 2)) * 64 + (lane & 3) * 8;
        bf16x8 wf[8];
#pragma unroll
        for (int i = 0; i < 8; ++i) wf[i] = ldtr2(wr + (i >> 1) * 2048 + (i & 1) * 32, wr + (i >> 1) * 2048 + (i & 1) * 32 + 256);
        if (z) {
#pragma unroll
          for (int i = 0; i < 8; ++i) acc[i & 1] = MFMA16(B8(xh[i >> 1]), wf[i], acc[i & 1]);
        } else {
#pragma unroll
          for (int i = 0; i < 8; ++i) acc[i & 1] = MFMA16(wf[i], B8(xh[i >> 1]), acc[i & 1]);
        }
        *(LAS f32x4*)((lds_ptr)lds + sl2 + lane * 16) = acc[0]; *(LAS f32x4*)((lds_ptr)lds + sl2 + 1024 + lane * 16) = acc[1]; }
      asm volatile("s_waitcnt lgkmcnt(0)\n\ts_barrier" ::: "memory");
      if (w == 0) {
#pragma unroll
        for (int ct = 0; ct < 2; ++ct) {
          f32x4 s = *(const LAS f32x4*)(L + R2 + ct * 1024 + lane * 16);
#pragma unroll
          for (int ww = 1; ww < 8; ++ww) s += *(const LAS f32x4*)(L + R2 + ww * 4096 + ct * 1024 + lane * 16);
          s *= z ? 1.f : QSCALE;
          const int d = ct * 16 + (lane & 15), rho = (d & 3) | (((d >> 3) & 1) << 2) | (((d >> 4) & 1) << 3) | (((d >> 2) & 1) << 4);
          const int pos = z ? (2 + (tt & 1)) * 64 + rho : ct * 64 + (tt & 1) * 16 + (lane & 15);
          asm volatile("s_nop 4\n\tglobal_store_dwordx2 %0, %1, %2 sc1" :: "v"((unsigned)((pos + 32 * (g & 1)) * 16 + (g >> 1) * 8)), "v"((u32x2){cvtpk(s[0], s[1]), cvtpk(s[2], s[3])}), "s"(kvf + (tt >> 1) * 4096) : "memory");
        }
      }
      asm volatile("s_waitcnt vmcnt(0)" : "+v"(wq4[0]), "+v"(wq4[1]), "+v"(wq4[2]), "+v"(wq4[3]), "+v"(wq4[4]), "+v"(wq4[5]), "+v"(wq4[6]), "+v"(wq4[7]),
                   "+v"(wq4[8]), "+v"(wq4[9]), "+v"(wq4[10]), "+v"(wq4[11]), "+v"(wq4[12]), "+v"(wq4[13]), "+v"(wq4[14]), "+v"(wq4[15]) :: "memory");
      if (tid == 0) raise(cnt + C_KV * 32 + blockIdx.x, tag);
      { f32x2 tq[4] = {}; float sh[16], sc[16];
        const LAS float* tp = (const LAS float*)(L + sl2 + 2048) + (lane >> 3);
#pragma unroll
        for (int e = 0; e < 16; ++e) { sh[e] = tp[e * 8]; sc[e] = tp[128 + e * 8]; }
        lds_ptr wd_ = (lds_ptr)lds + wbase + (lane >> 3) * 128 + (((lane & 7) ^ (((lane >> 4) & 1) << 2)) << 4);
#pragma unroll
        for (int e = 0; e < 16; ++e) { if (e < 12) HLOAD(e);
          u32x4 o;
#pragma unroll
          for (int j = 0; j < 4; ++j) { const f32x2 v = up2(wq4[e][j]); tq[j] += v * sh[e]; const f32x2 r = v * sc[e]; o[j] = cvtpk(r[0], r[1]); }
          *(LAS u32x4*)(wd_ + e * 1024) = o; SBAR(); }
        const float tv = fold8(tq, lane), th = rbf(tv);
        LAS bf16_t* ep = (LAS bf16_t*)((lds_ptr)lds + sl2 + 3584) + (lane & 7) * 8 + (lane >> 3);
        ep[0] = (bf16_t)(__float_as_uint(th) >> 16); ep[64] = (bf16_t)(cvtpk(tv - th, 0.f) & 0xffffu); }
      asm volatile("s_waitcnt lgkmcnt(0)\n\ts_barrier" ::: "memory");
    }
    STW(1);
#define KV_LD(c, reg, q) glds16c(kvf + (((c) + crot) & 7) * 32768 + w * 4096 + (q) * 1024, vlin, lds0 + (reg) + w * 4096 + (q) * 1024)
    u32x4 kvfl;
#define KVFLAG() GLOAD4C(kvfl, w == 0 ? (const char*)(cnt + C_KV * 32) : (const char*)cnd, vlin)
    f32x16 qa0 = {}, qa1 = {};
    {
      const int q = (lane & 15) >> 2, pp = lane & 3, nb = (lane >> 4) & 1;
      lds_cptr a0 = L + wk * R3 + hi * 1024 + q * 128 + ((wk ^ ((q >> 1) & 1)) * 64) + nb * 32 + pp * 8;
      lds_cptr a1 = L + wk * R3 + hi * 1024 + q * 128 + ((wk ^ 1 ^ ((q >> 1) & 1)) * 64) + nb * 32 + pp * 8;
      PIN2(a0, a1);
      { const lds_cptr e0 = L + R2 + q * 4096 + 3584 + hi * 128 + nb * 32 + pp * 8; const unsigned one = 0x3f803f80u; const u32x4 on = {wk ? 0u : one, wk ? 0u : one, wk ? one : 0u, wk ? one : 0u};
        qa0 = MFMA32(ldtr2(e0 + wk * 64, e0 + wk * 64 + 16384), B8(on), qa0); qa1 = MFMA32(ldtr2(e0 + (wk ^ 1) * 64, e0 + (wk ^ 1) * 64 + 16384), B8(on), qa1); PIN2(qa0, qa1); }
      bf16x8 w0 = ldtr2(a0, a0 + 512), w1 = ldtr2(a1, a1 + 512);
#define QSTEP(i, N, B, LOADS) do { if ((i) == 23) asm volatile("s_waitcnt vmcnt(" #N ") lgkmcnt(0)" : "+v"(hf[i]), "+v"(kvfl)); else asm volatile("s_waitcnt vmcnt(" #N ")" : "+v"(hf[i])); \
    if ((i) == 23 && w == 0) { if (__builtin_expect(!all4(kvfl, tag), 0)) await_packed(cnt + C_KV * 32, tag); if (blockIdx.x == 0 && lane == 0) raise(cnt + C_EPOCH * 32, tag); } if (B) BAR(); \
    bf16x8 n0 = w0, n1 = w1; if ((i) < 31) { n0 = ldtr2(a0 + ((i) + 1) * 2048, a0 + ((i) + 1) * 2048 + 512); n1 = ldtr2(a1 + ((i) + 1) * 2048, a1 + ((i) + 1) * 2048 + 512); } SBAR(); \
    qa0 = MFMA32(w0, B8(hf[i]), qa0); PIN(qa0); SBAR(); LOADS; qa1 = MFMA32(w1, B8(hf[i]), qa1); PIN(qa1); SBAR(); w0 = n0; w1 = n1; } while (0)
      QSTEP(0, 11, 0, HLOAD(12));
      QSTEP(1, 11, 0, HLOAD(13));
      QSTEP(2, 11, 0, HLOAD(14));
      QSTEP(3, 11, 0, HLOAD(15));
      QSTEP(4, 11, 0, HLOAD(16));
      QSTEP(5, 11, 0, HLOAD(17));
      QSTEP(6, 11, 0, HLOAD(18));
      QSTEP(7, 11, 0, HLOAD(19));
      QSTEP(8, 11, 0, HLOAD(20));
      QSTEP(9, 11, 0, HLOAD(21));
      QSTEP(10, 11, 0, HLOAD(22));
      QSTEP(11, 11, 0, HLOAD(23));
      QSTEP(12, 11, 0, HLOAD(24));
      QSTEP(13, 11, 0, KVFLAG(); HLOAD(25));
      QSTEP(14, 12, 0, HLOAD(26));
      QSTEP(15, 12, 0, HLOAD(27));
      QSTEP(16, 12, 0, HLOAD(28));
      QSTEP(17, 12, 0, HLOAD(29));
      QSTEP(18, 12, 0, HLOAD(30));
      QSTEP(19, 12, 0, HLOAD(31));
      QSTEP(20, 12, 0, (void)0);
      QSTEP(21, 11, 0, (void)0);
      QSTEP(22, 10, 0, (void)0);
      QSTEP(23, 7, 1, KV_LD(0, R0, 0));
      QSTEP(24, 9, 0, KV_LD(0, R0, 1));
      QSTEP(25, 8, 0, KV_LD(0, R0, 2));
      QSTEP(26, 8, 0, KV_LD(0, R0, 3));
      QSTEP(27, 8, 0, (void)0);
      QSTEP(28, 7, 0, (void)0);
      QSTEP(29, 6, 0, (void)0);
      QSTEP(30, 5, 0, (void)0);
      QSTEP(31, 4, 0, (void)0);
    }
    STW(2);
    bf16x8 qfa0, qfa1, qfb0, qfb1; float ma, mb;
    {
      lds_ptr xw = (lds_ptr)lds + R3 + w * 4096 + lane * 16; lds_cptr xr = L + R3 + (w ^ 4) * 4096 + lane * 16;
#pragma unroll
      for (int i = 0; i < 4; ++i) *(LAS f32x4*)(xw + i * 1024) = (f32x4){qa1[4 * i], qa1[4 * i + 1], qa1[4 * i + 2], qa1[4 * i + 3]};
      asm volatile("s_waitcnt vmcnt(0) lgkmcnt(0)\n\ts_barrier" ::: "memory");
      const bf16x8 k0 = ld128(L + lane * 16), k1 = ld128(L + lane * 16 + 1024);
#pragma unroll
      for (int i = 0; i < 4; ++i) { const f32x4 o = *(const LAS f32x4*)(xr + i * 1024);
#pragma unroll
        for (int j = 0; j < 4; ++j) qa0[4 * i + j] += o[j]; }
      qfa0 = pack8(qa0, 0); qfa1 = pack8(qa0, 1);
      const f32x16 z = {}; f32x16 s0 = MFMA32(k0, qfa0, z); s0 = MFMA32(k1, qfa1, s0); PIN(s0);
      *(LAS bf16x8*)(xw + 32768) = qfa0; *(LAS bf16x8*)(xw + 32768 + 1024) = qfa1;
      KV_LD(1, R2, 0); KV_LD(1, R2, 1); KV_LD(1, R2, 2); KV_LD(1, R2, 3);
      ma = rowmax16(s0);
      *(LAS float*)(xw + 32768 + 2048) = ma;
      asm volatile("s_waitcnt lgkmcnt(0)\n\ts_barrier" ::: "memory");
      qfb0 = ld128(xr + 32768); qfb1 = ld128(xr + 32768 + 1024); mb = *(const LAS float*)(xr + 32768 + 2048);
    }
    u32x4 sel;
    { const unsigned on = (lane & 2) ? 0u : 0x3f803f80u; const bool od = (lane >> 2) & 1; sel = (u32x4){od ? 0u : on, od ? 0u : on, od ? on : 0u, od ? on : 0u}; }
    const int sidx = (lane & 1) ? 0xEEEE : 0x4444;
    f32x16 oa = {}, ob = {}; f32x4 la = {}, lb = {};
    const lds_cptr kv0 = L + wk * 16384 + lane * 16;
    bf16x8 kf0 = ld128(kv0), kf1 = ld128(kv0 + 1024), vf0 = ld128(kv0 + 2048), vf1 = vf0;
    f32x16 sA, sB, na, nb;
#pragma unroll
    for (int r = 0; r < 16; ++r) { na[r] = -ma; nb[r] = -mb; }
    PIN2(na, nb);
    sA = MFMA32(kf0, qfa0, na); sA = MFMA32(kf1, qfa1, sA);
    u32x8 pA, pB = {0, 0, 0, 0, 0, 0, 0, 0};
    const unsigned ovoff = (unsigned)((w * 8 + (lane >> 3)) * 2048 + tn * 128) + wimg_src_chunk(lane);
#define REG(c) ((c) % 3 == 0 ? R0 : (c) % 3 == 1 ? R2 : R1)
#define SIDE(cw, k) do { if ((cw) >= 0 && (cw) < 6 && (k) < 4) KV_LD((cw) + 2, REG((cw) + 2), k); \
    else if (((cw) == 0 || (cw) == 1) && (k) >= 4) WTST(((cw) * 2 + (k)) & 3);       \
    else if (((cw) == 2 || (cw) == 3) && (k) == 4) { const int ln_ = bi * 256 + w * 32 + ((cw) & 1) * 16 + (lane & 15); TOUCH255(wo, (unsigned)((ln_ >> 3) * 2048 + (xcd & 1) * 1024 + (ln_ & 7) * 128)); } \
    else if (((cw) == 4 || (cw) == 5) && (k) >= 4) { OWL((cw) - 4, ((k) & 1) * 2, ovoff); OWL((cw) - 4, ((k) & 1) * 2 + 1, ovoff); } \
    else if ((cw) == 6 && (k) < 4) OWL(2, k, ovoff); \
    else if ((cw) == 6 && (k) == 4) TOUCH255((const char*)x + (size_t)(tm * 128 + w * 16) * 2048 + tn * 128, (unsigned)((lane & 15) * 2048)); \
    if (GUPF && (k) == 5 && (cw) >= 1) TOUCH255((const char*)((cw) < 4 ? wg : wu) + (((cw) - 1) % 3) * 1152, gvoff); } while (0)
    const unsigned gvoff = (unsigned)(((blockIdx.x * 8 + w) * 28 + (lane < 10 ? lane : 9)) * 128);
#define EX(S, r) S[r] = __builtin_amdgcn_exp2f(S[r])
#define CV(P, S, r) P[(r) >> 1] = cvtpk(S[r], S[(r) + 1])
#define HS(SC, SN, PP, PC, q0, q1, nm, oac, lac, L0, L1, L2, L3, G) do { \
    SN = MFMA32(kf0, q0, nm); PIN(SN); SBAR(); L0; EX(SC, 0); EX(SC, 1); EX(SC, 2); EX(SC, 3); PIN(SC); SBAR(); \
    SN = MFMA32(kf1, q1, SN); PIN(SN); SBAR(); L1; EX(SC, 4); EX(SC, 5); CV(PC, SC, 0); CV(PC, SC, 2); EX(SC, 6); PIN2(SC, PC); SBAR(); \
    oac = MFMA32(vf0, plo(PP), oac); PIN(oac); SBAR(); L2; EX(SC, 7); EX(SC, 8); CV(PC, SC, 4); EX(SC, 9); CV(PC, SC, 6); PIN2(SC, PC); SBAR(); \
    oac = MFMA32(vf1, phi(PP), oac); PIN(oac); SBAR(); L3; EX(SC, 10); EX(SC, 11); CV(PC, SC, 8); EX(SC, 12); CV(PC, SC, 10); PIN2(SC, PC); SBAR(); \
    lac = ROWSUM(sel, PP, lac, sidx); PIN(lac); SBAR(); G; EX(SC, 13); EX(SC, 14); EX(SC, 15); CV(PC, SC, 12); CV(PC, SC, 14); PIN(PC); SBAR(); } while (0)
#define STEP(pc, pn, G0, G1) do { HS(sA, sB, pB, pA, qfb0, qfb1, nb, ob, lb, kf0 = ld128(pn), kf1 = ld128((pn) + 1024), vf0 = ld128((pc) + 2048), vf1 = ld128((pc) + 3072), G0); \
    HS(sB, sA, pA, pB, qfa0, qfa1, na, oa, la, (void)0, (void)0, (void)0, (void)0, G1); } while (0)
    const int ru = (blockIdx.x < 224 ? blockIdx.x : blockIdx.x - 224) * 8 + w, rJ = ru >> 4, rt = ru & 15;
#pragma unroll
    for (int i = 0; i < 4; ++i) glds16x((const char*)wd + (size_t)(rJ * 32 + i * 8) * 2048 + rt * 128, (unsigned)((lane >> 3) * 2048) + wimg_src_chunk(lane), lds0 + R3 + w * 4096 + i * 1024);
#define WTST(i) do { const lds_cptr p_ = L + R3 + w * 4096 + wimg_rd(lane, i); \
      asm volatile("s_nop 4\n\tglobal_store_dwordx4 %0, %1, %2 sc1\n\ts_nop 1" :: "v"(vlin), "v"(ldtr2(p_, p_ + 2048)), "s"(wdt + (size_t)((rt * 112 + rJ) * 4 + (i)) * 1024) : "memory"); } while (0)
    STW(3);
    lds_cptr kvr[3] = {kv0 + R0, kv0 + R2, kv0 + R1};
    PIN2(kvr[1], kvr[2]);
#define CHUNK(c) do { const lds_cptr pc = kvr[(c) % 3], pn = (c) < 7 ? kvr[((c) + 1) % 3] : pc + 12288; \
      __builtin_amdgcn_s_setprio(2); STEP(pc, pc + 4096, SIDE((c) - 1, 2), SIDE((c) - 1, 3)); \
      __builtin_amdgcn_s_setprio(1); STEP(pc + 4096, pc + 8192, SIDE((c) - 1, 4), SIDE((c) - 1, 5)); \
      __builtin_amdgcn_s_setprio(0); STEP(pc + 8192, pc + 12288, (void)0, (void)0); \
      if ((c) == 0) WAITV(0); \
      else if ((c) < 7) asm volatile("s_waitcnt vmcnt(%0)" :: "n"(((c) < 3 ? 2 : (c) < 5 ? 1 : 4) + (GUPF && (c) >= 2)) : "memory"); \
      BAR();                                        \
      __builtin_amdgcn_s_setprio(3); \
      if ((c) == 4 && tid == 0) raise(cnt + C_RT * 32 + blockIdx.x, tag);        \
      SBAR(); STEP(pc + 12288, pn, SIDE(c, 0), SIDE(c, 1)); } while (0)
    CHUNK(0); CHUNK(1); CHUNK(2); CHUNK(3); CHUNK(4); CHUNK(5); CHUNK(6); CHUNK(7);
    __builtin_amdgcn_s_setprio(0);
    ob = MFMA32(vf0, plo(pB), ob); ob = MFMA32(vf1, phi(pB), ob); lb = ROWSUM(sel, pB, lb, sidx);
    STW(4);
    float lsum;
    {
      lds_ptr xb = (lds_ptr)lds + R0;
#pragma unroll
      for (int i = 0; i < 4; ++i) *(LAS f32x4*)(xb + w * 4096 + i * 1024 + lane * 16) = (f32x4){ob[4 * i], ob[4 * i + 1], ob[4 * i + 2], ob[4 * i + 3]};
      *(LAS float*)((lds_ptr)lds + R2 + w * 256 + lane * 4) = lb[0] + lb[1];
      asm volatile("s_waitcnt lgkmcnt(0)\n\ts_barrier" ::: "memory");
      lsum = (la[0] + la[1]) + *(const LAS float*)(L + R2 + (w ^ 4) * 256 + lane * 4);
#pragma unroll
      for (int i = 0; i < 4; ++i) { const f32x4 o = *(const LAS f32x4*)(xb + (w ^ 4) * 4096 + i * 1024 + lane * 16);
#pragma unroll
        for (int j = 0; j < 4; ++j) oa[4 * i + j] += o[j]; }
    }
    float amax = fabsf(oa[0]);
#pragma unroll
    for (int r = 1; r < 16; ++r) amax += fabsf(oa[r]);
    if (__any(!(lsum < 1.0e12f) || !(amax < 3.0e38f))) {
      float m = -INFINITY; lsum = 0.f;
#pragma unroll
      for (int r = 0; r < 16; ++r) oa[r] = 0.f;
#pragma unroll 1
      for (int t = 0; t < 64; ++t) {
        const f32x16 z = {}; u32x4 f0, f1, f2, f3;
        asm volatile("s_nop 4\n\tglobal_load_dwordx4 %0, %4, %5 sc1\n\tglobal_load_dwordx4 %1, %4, %5 offset:1024 sc1\n\tglobal_load_dwordx4 %2, %4, %5 offset:2048 sc1\n\t"
                     "global_load_dwordx4 %3, %4, %5 offset:3072 sc1\n\ts_waitcnt vmcnt(0)" : "=&v"(f0), "=&v"(f1), "=&v"(f2), "=&v"(f3) : "v"(vlin), "s"(kvf + t * 4096) : "memory");
        f32x16 s = MFMA32(B8(f0), qfa0, z); s = MFMA32(B8(f1), qfa1, s);
        const float mn = fmaxf(m, rowmax16(s)), al = __builtin_amdgcn_exp2f(m - mn); m = mn;
#pragma unroll
        for (int r = 0; r < 16; ++r) { s[r] = __builtin_amdgcn_exp2f(s[r] - mn); oa[r] *= al; }
        u32x8 p;
#pragma unroll
        for (int r = 0; r < 16; r += 2) CV(p, s, r);
        f32x4 lt = {}; lt = ROWSUM(sel, p, lt, sidx);
        lsum = lsum * al + (lt[0] + lt[1]);
        oa = MFMA32(B8(f2), plo(p), oa); oa = MFMA32(B8(f3), phi(p), oa);
      }
    }
    const float rl = __builtin_amdgcn_rcpf(lsum);
#pragma unroll
    for (int r = 0; r < 16; ++r) oa[r] *= rl;
    char* op = ofm + ((size_t)((tn * 2 + wk) * 128 + tm * 8 + wm * 2 + ((lane >> 4) & 1)) * 64 + (lane & 15) + 32 * hi) * 16;
    store16(op, __builtin_bit_cast(u32x4, pack8(oa, 0))); store16(op + 256, __builtin_bit_cast(u32x4, pack8(oa, 1)));
    asm volatile("s_waitcnt vmcnt(0)\n\ts_barrier" ::: "memory");
    if (tid == 0) raise(cnt + (C_O + tm * 16 + tn) * 32, tag);
  }
  STW(5);
  const int tm5 = (xcd & 1) * 4 + (bi & 3), tn5 = (xcd >> 1) * 7 + (bi >> 2);
  const char* sB = (const char*)(w < 4 ? wg : wu) + (size_t)(tn5 * 128 + ((w >> 1) & 1) * 64) * 2 + (size_t)(16 * (w & 1)) * 7168;
#define K5_B(s, slot, vB) do { const unsigned d_ = lds0 + (slot) * 32768 + 16384 + w * 2048; glds16w(sB + (size_t)(s) * 32 * 7168, vB, d_); glds16w(sB + (size_t)(s) * 32 * 7168 + 8 * 7168, vB, d_ + 1024); } while (0)
  u32x4 x1p[2];
  {
    IDS();
    const unsigned ovoff = (unsigned)((w * 8 + (lane >> 3)) * 2048 + tn * 128) + wimg_src_chunk(lane);
    const unsigned xvoff = (unsigned)((lane >> 3) * 2048 + (((lane & 7) ^ (lane >> 3)) << 4));
#define OLATE() do { _Pragma("unroll") for (int k = 0; k < 2; ++k) glds16((const char*)x + (size_t)(tm * 128 + wm * 32 + wk * 16 + k * 8) * 2048 + tn * 128, xvoff, lds0 + R0 + w * 4096 + k * 1024); \
    OWL(3, 0, ovoff); OWL(3, 1, ovoff); OWL(3, 2, ovoff); OWL(3, 3, ovoff); } while (0)
    OLATE();
    u32x4 af[16][2];
    const char* ab = ofm + (size_t)(wk * 16 * 128 + tm * 8 + wm * 2) * 1024;
    const unsigned va0 = vlin + wk * 1024, va1 = vlin + (wk ^ 1) * 1024;
#define AL(i, h) GLOAD4C(af[i][h], ab + (size_t)((i) ^ orot) * 131072, (h) ? va1 : va0)
    f32x4 acc[2][4] = {};
    lds_cptr a[2][4];
#pragma unroll
    for (int t = 0; t < 4; ++t) { a[0][t] = L + R3 + wk * 16384 + wimg_rd(lane, t); a[1][t] = L + R1 + wk * 16384 + wimg_rd(lane, t); PIN2(a[0][t], a[1][t]); PIN2(acc[0][t], acc[1][t]); }
#define OFR(i, t) ldtr2(a[(i) >> 3][t] + (((i) >> 2) & 1) * 32768 + ((i) & 3) * 4096, a[(i) >> 3][t] + (((i) >> 2) & 1) * 32768 + ((i) & 3) * 4096 + 2048)
    bf16x8 wf[4], wn[4];
#pragma unroll
    for (int t = 0; t < 4; ++t) wf[t] = OFR(0, t);
    if (w == 0) await_n<2>(cnt + (C_O + tm * 16) * 32, 16, tag); else if (w == 1) await_packed(cnt + C_RT * 32, tag);
    else if (w == 2) {
      const f32x4 m = rbf4(gather_mod(modp, (unsigned)((2048 + tn * 64) * 8 + (lane & 31) * 16), (unsigned)((3072 + tn * 64) * 8 + (lane & 31) * 16), tag) + (f32x4){0.f, 0.f, 1.f, 1.f});
      lds_ptr p = (lds_ptr)lds + R0 + (lane >> 5) * 4096 + 2048 + (lane & 31) * 8;
#pragma unroll
      for (int k = 0; k < 4; ++k) { *(LAS f32x2*)(p + k * 8192) = (f32x2){m[0], m[1]}; *(LAS f32x2*)(p + k * 8192 + 1024) = (f32x2){m[2], m[3]}; } }
    asm volatile("s_waitcnt vmcnt(0) lgkmcnt(0)\n\ts_barrier" ::: "memory");
    STW(6);
    AL(0, 0); AL(0, 1); AL(1, 0); AL(1, 1); AL(2, 0); AL(2, 1);
    const int g = lane >> 4;
    lds_ptr xb = (lds_ptr)lds + R0;
    u32x2 xr[4]; f32x4 gav[4], cbv[4];
    asm volatile("" ::: "memory");
#pragma unroll
    for (int t = 0; t < 4; ++t) { const lds_cptr e = xb + w * 4096;
      xr[t] = *(const LAS u32x2*)(e + (lane & 15) * 128 + (((2 * t + (g >> 1)) ^ (lane & 7)) << 4) + (g & 1) * 8);
      gav[t] = *(const LAS f32x4*)(e + 2048 + t * 64 + g * 16); cbv[t] = *(const LAS f32x4*)(e + 3072 + t * 64 + g * 16); }
    asm volatile("s_waitcnt lgkmcnt(0)" : "+v"(xr[0]), "+v"(xr[1]), "+v"(xr[2]), "+v"(xr[3]), "+v"(gav[0]), "+v"(gav[1]), "+v"(gav[2]), "+v"(gav[3]),
                 "+v"(cbv[0]), "+v"(cbv[1]), "+v"(cbv[2]), "+v"(cbv[3]) :: "memory");
    {
#define OMM(i, t) do { acc[0][t] = MFMA16(wf[t], __builtin_bit_cast(bf16x8, af[i][0]), acc[0][t]); acc[1][t] = MFMA16(wf[t], __builtin_bit_cast(bf16x8, af[i][1]), acc[1][t]); PIN2(acc[0][t], acc[1][t]); SBAR(); } while (0)
#define OSTEP(i, N, L0, L1, L2, L3) do { asm volatile("s_waitcnt vmcnt(" #N ")" : "+v"(af[i][0]), "+v"(af[i][1])); \
    _Pragma("unroll") for (int t = 0; t < 4; ++t) wn[t] = (i) < 15 ? OFR(((i) + 1) & 15, t) : wf[t]; SBAR(); \
    OMM(i, 0); L0; OMM(i, 1); L1; OMM(i, 2); L2; OMM(i, 3); L3; _Pragma("unroll") for (int t = 0; t < 4; ++t) wf[t] = wn[t]; } while (0)
      OSTEP(0, 4, AL(3, 0), (void)0, AL(3, 1), (void)0);
      OSTEP(1, 4, AL(4, 0), (void)0, AL(4, 1), (void)0);
      OSTEP(2, 4, AL(5, 0), (void)0, AL(5, 1), (void)0);
      OSTEP(3, 4, AL(6, 0), (void)0, AL(6, 1), (void)0);
      OSTEP(4, 4, AL(7, 0), (void)0, AL(7, 1), (void)0);
      OSTEP(5, 4, AL(8, 0), (void)0, AL(8, 1), (void)0);
      OSTEP(6, 4, AL(9, 0), (void)0, AL(9, 1), (void)0);
      OSTEP(7, 4, AL(10, 0), (void)0, AL(10, 1), (void)0);
      OSTEP(8, 4, AL(11, 0), (void)0, AL(11, 1), (void)0);
      OSTEP(9, 4, AL(12, 0), (void)0, AL(12, 1), (void)0);
      OSTEP(10, 4, AL(13, 0), (void)0, AL(13, 1), (void)0);
      OSTEP(11, 4, AL(14, 0), (void)0, AL(14, 1), (void)0);
      OSTEP(12, 4, AL(15, 0), (void)0, AL(15, 1), (void)0);
      OSTEP(13, 4, (void)0, (void)0, (void)0, (void)0);
      OSTEP(14, 2, (void)0, (void)0, (void)0, (void)0);
      OSTEP(15, 0, (void)0, (void)0, (void)0, (void)0);
    }
    STW(7);
#pragma unroll
    for (int t = 0; t < 4; ++t) *(LAS f32x4*)(xb + w * 4096 + t * 1024 + lane * 16) = acc[1][t];
    asm volatile("s_waitcnt lgkmcnt(0)\n\ts_barrier" ::: "memory");
    f32x4 v[4];
#pragma unroll
    for (int t = 0; t < 4; ++t) {
      const f32x4 o = *(const LAS f32x4*)(xb + (w ^ 4) * 4096 + t * 1024 + lane * 16) + acc[0][t];
      const f32x4 xv = {bflo(xr[t][0]), bfhi(xr[t][0]), bflo(xr[t][1]), bfhi(xr[t][1])}, r = xv + gav[t] * o;
      const unsigned p0 = cvtpk(r[0], r[1]), p1 = cvtpk(r[2], r[3]);
      x1p[t >> 1][(t & 1) * 2] = p0; x1p[t >> 1][(t & 1) * 2 + 1] = p1;
      v[t] = (f32x4){bflo(p0), bfhi(p0), bflo(p1), bfhi(p1)};
    }
    char* yp = yfm + (size_t)(tn * 2 * 128 + tm * 8 + wm * 2 + wk) * 1024 + lane * 16;
#pragma unroll
    for (int jj = 0; jj < 2; ++jj) { const f32x4 y0 = v[2 * jj] * cbv[2 * jj], y1 = v[2 * jj + 1] * cbv[2 * jj + 1];
      store16(yp + jj * 131072, (u32x4){cvtpk(y0[0], y0[1]), cvtpk(y0[2], y0[3]), cvtpk(y1[0], y1[1]), cvtpk(y1[2], y1[3])}); }
    const f32x4 s4 = (v[0] + v[1]) + (v[2] + v[3]);
    const float mw = sum4g((s4[0] + s4[1]) + (s4[2] + s4[3])) * (1.f / 64.f);
    f32x4 q4 = {0.f, 0.f, 0.f, 0.f};
#pragma unroll
    for (int t = 0; t < 4; ++t) { const f32x4 d = v[t] - mw; q4 += d * d; }
    const float q = sum4g((q4[0] + q4[1]) + (q4[2] + q4[3]));
    if (g == 0) __hip_atomic_store((unsigned long long*)(stat + (((tm * 8 + wm * 2 + wk) * 16 + tn) * 16 + (lane & 15)) * 8),
                                   ((unsigned long long)__float_as_uint(q) << 32) | __float_as_uint(mw), __ATOMIC_RELAXED, __HIP_MEMORY_SCOPE_AGENT);
    asm volatile("s_waitcnt vmcnt(0)\n\ts_barrier" : "+v"(x1p[0]), "+v"(x1p[1]) :: "memory");
    if (tid == 0) raise(cnt + (C_Y + tm * 16 + tn) * 32, tag);
  }
  STW(8);
  const char* wp = wdt + (size_t)(tn * 448 + (tn & 1) * 248 + w) * 1024;
  const int wo2 = (tn & 1) ? -448 * 1024 : 448 * 1024;
#define DW(u, h) glds16f(wp + ((h) ? wo2 : 0), vlin, lds0 + (u) * 16384 + (h) * 8192 + w * 1024)
#define DW4() do { asm volatile("s_nop 4"); DW(0, 0); DW(0, 1); wp += 8192; DW(1, 0); DW(1, 1); wp += 8192; DW(2, 0); DW(2, 1); wp += 8192; DW(3, 0); DW(3, 1); } while (0)
  if (bi < 28) {
    IDS();
    const int wn = wk;
    const char* sA = yfm + (size_t)(tm5 * 16 + 2 * w) * 1024;
    const unsigned vB = (unsigned)((lane >> 3) * 7168) + wimg_src_chunk(lane);
#define K5_A(s, slot) do { const unsigned d_ = lds0 + (slot) * 32768 + w * 2048; glds16f(sA + (size_t)(s) * 131072, vlin, d_); glds16f(sA + (size_t)(s) * 131072 + 1024, vlin, d_ + 1024); } while (0)
    K5_B(0, 1, vB); K5_B(1, 2, vB); K5_B(2, 3, vB);
    lds_cptr ra[2], rb[2][2];
#pragma unroll
    for (int h = 0; h < 2; ++h) { ra[h] = L + h * 65536 + wn * 8192 + lane * 16; PIN(ra[h]);
#pragma unroll
      for (int t = 0; t < 2; ++t) { rb[h][t] = L + h * 65536 + 16384 + (wm >> 1) * 4096 + wimg_rd(lane, 2 * (wm & 1) + t); PIN(rb[h][t]); } }
    f32x4 ag[8][2] = {}, au[8][2] = {};
#pragma unroll
    for (int m = 0; m < 8; ++m) asm volatile("" : "+v"(ag[m][0]), "+v"(ag[m][1]), "+v"(au[m][0]), "+v"(au[m][1]));
    if (w == 0) await_n<2>(cnt + (C_Y + tm5 * 32) * 32, 32, tag);
    BAR();
    STW(9);
    asm volatile("s_nop 4");
    K5_A(0, 1); K5_A(1, 2); K5_A(2, 3);
    asm volatile("s_waitcnt vmcnt(4)\n\ts_barrier" ::: "memory");
    STW(10);
    if (wn) BAR();
    const unsigned* fl5 = cnt + ((lane & 1) ? C_SIDE + tn5 : C_RS + (tm5 >> 1)) * 32;
#define K5_STEP(u, sn, LAST) do { constexpr int slot = ((u) + 1) & 3; \
    if ((u) == 0 && __builtin_expect(s == 24 && w == 0, 0)) glds16c(cnt, (unsigned)((const char*)fl5 - (const char*)cnt), lds0 + R4 + 4096); \
    if ((u) == 3 && __builtin_expect(s == 24 && w == 0, 0)) { if (!__all(*(const LAS unsigned*)(L + R4 + 4096 + lane * 16) == tag)) await<8>(fl5, tag); } \
    if ((u) == 0 && __builtin_expect(s == 28 && w < 4, 0)) glds16c((w < 2 ? utp + tn5 * 2048 : ws + WS_RS + tm5 * 2048 - 2048) + w * 1024, vlin, lds0 + R4 + w * 1024); \
    bf16x8 xa[8], wgf[2], wuf[2]; \
    _Pragma("unroll") for (int t = 0; t < 2; ++t) { const lds_cptr p_ = rb[slot >> 1][t] + (slot & 1) * 32768; wgf[t] = ldtr2(p_, p_ + 2048); wuf[t] = ldtr2(p_ + 8192, p_ + 8192 + 2048); } \
    _Pragma("unroll") for (int m = 0; m < 8; ++m) xa[m] = ld128(ra[slot >> 1] + (slot & 1) * 32768 + m * 1024); \
    if (__builtin_expect((sn) < 32, 1)) { K5_A(sn, u); K5_B(sn, u, vB); WAITV(8); } else WAITV(0); \
    if ((u) == 0 && __builtin_expect(s == 0, 0)) WAITV(6); \
    asm volatile("s_waitcnt lgkmcnt(0)\n\ts_barrier" ::: "memory"); SBAR(); \
    __builtin_amdgcn_s_setprio(1); \
    _Pragma("unroll") for (int e = 0; e < 32; ++e) { const int t = e >> 4, m = (e & 8) ? 7 - (e & 7) : (e & 7); \
      if (e == 31 - EARLYB && EARLYB) asm volatile("s_setprio 2"); if (e == 32 - EARLYB && __builtin_expect(!(LAST), 1)) BAR();        \
      if (e & 8) MFMA16A(au[m][t], wuf[t], xa[m]); else MFMA16A(ag[m][t], wgf[t], xa[m]); } \
    __builtin_amdgcn_s_setprio(0); SBAR(); \
    if (EARLYB == 0 && __builtin_expect(!(LAST), 1)) BAR(); } while (0)
#pragma unroll 1
    for (int s = 0; s < 32; s += 4) {
      K5_STEP(0, s + 3, false);
      K5_STEP(1, s + 4, false);
      K5_STEP(2, s + 5, false);
      K5_STEP(3, s + 6, s == 28 && wn);
    }
    asm volatile("s_nop 15");
    STW(11);
    DW4();
    f32x4 tb[2][4]; f32x2 rr[8];
#pragma unroll
    for (int tt = 0; tt < 2; ++tt)
#pragma unroll
      for (int k = 0; k < 4; ++k) tb[tt][k] = *(const LAS f32x4*)(L + R4 + (k * 128 + wm * 32 + tt * 16 + 4 * (lane >> 4)) * 4);
#pragma unroll
    for (int m = 0; m < 8; ++m) rr[m] = *(const LAS f32x2*)(L + R4 + 2048 + (wn * 128 + m * 16 + (lane & 15)) * 8);
#pragma unroll
    for (int m = 0; m < 8; ++m) {
      u32x4 o;
#pragma unroll
      for (int e = 0; e < 4; ++e) {
#define H2(v) (f32x2){(v)[2 * (e & 1)], (v)[2 * (e & 1) + 1]}
        const f32x2 a = H2(ag[m][e >> 1]) * rr[m][0] + (H2(tb[e >> 1][0]) * rr[m][1] + H2(tb[e >> 1][1]));
        const f32x2 u = H2(au[m][e >> 1]) * rr[m][0] + (H2(tb[e >> 1][2]) * rr[m][1] + H2(tb[e >> 1][3]));
        const f32x2 z = a * ((a * a) * (-0.044715f * 2.302208198f) + (-2.302208198f));
        const f32x2 ex = {__builtin_amdgcn_exp2f(z[0]), __builtin_amdgcn_exp2f(z[1])};
        const f32x2 dn = ex + 1.f;
        const f32x2 rc = {__builtin_amdgcn_rcpf(dn[0]), __builtin_amdgcn_rcpf(dn[1])};
        const f32x2 r = (a * u) * rc;
        o[e] = cvtpk(r[0], r[1]);
      }
      asm volatile("s_nop 4\n\tglobal_store_dwordx4 %0, %1, %2 offset:%3 sc1\n\ts_nop 1" :: "v"(vlin), "v"(o), "s"(actfm + (size_t)((tn5 * 4 + wm) * 128 + tm5 * 16 + wn * 8 + (m & 4)) * 1024), "n"((m & 3) * 1024) : "memory");
      SBAR();
    }
    asm volatile("s_waitcnt vmcnt(0)\n\ts_barrier" ::: "memory");
    if (tid == 0) raise(cnt + (C_ACT + tm5 * 32 + tn5) * 32, tag);
  } else {
    IDS();
    const int sp = xcd * 4 + bi - 28, g = lane >> 4, j8 = sp & 7, tu = (xcd >> 1) * 7 + j8;
    float* vecf = (float*)(ws + WS_GBF);
    LAS float* red = (LAS float*)((lds_ptr)lds + R4);
#define MODVEC(col0, k0, s) do { f32x2 acc[4] = {}; u32x4 v[8]; const u32x4 cv = *(const u32x4*)(cnd + (k0) + (tid >> 3) * 8); \
      _Pragma("unroll") for (int i = 0; i < 8; ++i) v[i] = __builtin_nontemporal_load((const u32x4*)(wmod + (size_t)((k0) + (tid >> 3) * 8 + i) * NMOD + (col0) + (tid & 7) * 8)); \
      SBAR(); \
      _Pragma("unroll") for (int i = 0; i < 8; ++i) { const float sv = silu_bf((i & 1) ? bfhi(cv[i >> 1]) : bflo(cv[i >> 1])); _Pragma("unroll") for (int j = 0; j < 4; ++j) acc[j] += up2(v[i][j]) * sv; } \
      asm volatile("s_waitcnt lgkmcnt(0)\n\ts_barrier" ::: "memory"); \
      red[w * 64 + (lane & 7) * 8 + (lane >> 3)] = fold8(acc, lane); \
      asm volatile("s_waitcnt lgkmcnt(0)\n\ts_barrier" ::: "memory"); \
      _Pragma("unroll") for (int ww = 0; ww < 8; ++ww) s += red[ww * 64 + lane]; } while (0)
    { float s = 0.f; MODVEC(3072 + (sp >> 1) * 64, (sp & 1) * 512, s);
      if (w == 0) { __hip_atomic_store(vecf + 2048 + (sp & 1) * 1024 + (sp >> 1) * 64 + lane, s, __ATOMIC_RELAXED, __HIP_MEMORY_SCOPE_AGENT); WAITV(0); if (lane == 0) raise(cnt + (C_SB + sp) * 32, tag); } }
    if (j8 == 7) {
      const int q4 = xcd >> 1;
      if (w == 0) await<8>(cnt + (C_Y + q4 * 64 + lane) * 32, tag);
      BAR();
      f32x2 st[16]; float ms = 0.f, q = 0.f;
#pragma unroll
      for (int j = 0; j < 16; ++j) { const unsigned long long v = __hip_atomic_load((const unsigned long long*)(stat + q4 * 65536 + (tid >> 4) * 2048 + j * 128 + (tid & 15) * 8), __ATOMIC_RELAXED, __HIP_MEMORY_SCOPE_AGENT);
        st[j] = (f32x2){__uint_as_float((unsigned)v), __uint_as_float((unsigned)(v >> 32))}; }
#pragma unroll
      for (int j = 0; j < 16; ++j) ms += st[j][0];
      ms *= (1.f / 16.f);
#pragma unroll
      for (int j = 0; j < 16; ++j) { const float d = st[j][0] - ms; q += st[j][1] + 64.f * d * d; }
      const float r = __builtin_amdgcn_rsqf(q * (1.f / D_) + LN_EPS);
      __hip_atomic_store((unsigned long long*)(ws + WS_RS) + q4 * 512 + tid, ((unsigned long long)__float_as_uint(-r * ms) << 32) | __float_as_uint(r), __ATOMIC_RELAXED, __HIP_MEMORY_SCOPE_AGENT);
      asm volatile("s_waitcnt vmcnt(0)\n\ts_barrier" ::: "memory");
      if (tid == 0) raise(cnt + (C_RS + q4) * 32, tag);
    } else {
      const unsigned uvoff = (unsigned)((lane >> 3) * 7168) + wimg_src_chunk(lane);
      const char* us = (const char*)(w < 4 ? wg : wu) + (tu * 128 + ((w >> 1) & 1) * 64) * 2 + (size_t)((w & 1) * 64) * 7168;
#define UT_LD(i) do { _Pragma("unroll") for (int q = 0; q < 8; ++q) glds16(us + (size_t)((((i) + 6) & 7) * 128 + q * 8) * 7168, uvoff, lds0 + w * 16384 + ((i) & 1) * 8192 + q * 1024); } while (0)
      UT_LD(0); UT_LD(1);
      await_n<8>(cnt + C_SB * 32, 32, tag);
      { const int k = tid * 2; const f32x4 cb = gather_mod(modp, (unsigned)((3072 + k) * 8), (unsigned)((3072 + k) * 8), tag);
        const unsigned long long s0 = __hip_atomic_load((const unsigned long long*)(vecf + 2048 + k), __ATOMIC_RELAXED, __HIP_MEMORY_SCOPE_AGENT);
        const unsigned long long s1 = __hip_atomic_load((const unsigned long long*)(vecf + 3072 + k), __ATOMIC_RELAXED, __HIP_MEMORY_SCOPE_AGENT);
        lds_ptr cp = (lds_ptr)lds + R4 + 4096 + (k >> 5) * 128 + ((k >> 2) & 3) * 32 + ((k >> 4) & 1) * 8 + (k & 2) * 2;
        *(LAS unsigned*)cp = cvtpk(cb[0] + 1.f, cb[1] + 1.f);
        *(LAS unsigned*)(cp + 16) = cvtpk(__uint_as_float((unsigned)s0) + __uint_as_float((unsigned)s1), __uint_as_float((unsigned)(s0 >> 32)) + __uint_as_float((unsigned)(s1 >> 32))); }
      asm volatile("s_waitcnt lgkmcnt(0)\n\ts_barrier" ::: "memory");
      f32x4 ua[4] = {};
#pragma unroll 1
      for (int i = 0; i < 8; ++i) {
        if (i < 7) WAITV(8); else WAITV(0);
        const lds_cptr cr = L + R4 + 4096 + (((i + 6) & 7) * 4 + (w & 1) * 2) * 128 + g * 32 + (lane & 1) * 16;
#pragma unroll
        for (int J = 0; J < 2; ++J)
#pragma unroll
          for (int t = 0; t < 4; ++t) { const lds_cptr wr = L + w * 16384 + (i & 1) * 8192 + J * 4096 + wimg_rd(lane, t); ua[t] = MFMA16(ldtr2(wr, wr + 2048), ld128(cr + J * 128), ua[t]); }
        asm volatile("s_waitcnt lgkmcnt(0)" : "+v"(ua[0]), "+v"(ua[1]), "+v"(ua[2]), "+v"(ua[3]) :: "memory");
        if (i < 6) UT_LD(i + 2);
      }
      lds_ptr xb = (lds_ptr)lds + R4 + 8192 + (w >> 1) * 4096 + lane * 16;
      if (w & 1) {
#pragma unroll
        for (int t = 0; t < 4; ++t) *(LAS f32x4*)(xb + t * 1024) = ua[t];
      }
      asm volatile("s_waitcnt lgkmcnt(0)\n\ts_barrier" ::: "memory");
      if (!(w & 1) && (lane & 15) < 2) {
#pragma unroll
        for (int t = 0; t < 4; ++t) store16(utp + ((tu * 4 + (w >> 2) * 2 + (lane & 1)) * 128 + ((w >> 1) & 1) * 64 + 16 * t + 4 * g) * 4, __builtin_bit_cast(u32x4, ua[t] + *(const LAS f32x4*)(xb + t * 1024)));
      }
      asm volatile("s_waitcnt vmcnt(0)\n\ts_barrier" ::: "memory");
      if (tid == 0) raise(cnt + (C_SIDE + tu) * 32, tag);
    }
    { float s = 0.f; MODVEC(5120 + (sp >> 1) * 64, (sp & 1) * 512, s);
      if (w == 0) { __hip_atomic_store(vecf + (sp >> 1) * 128 + (sp & 1) * 64 + lane, s, __ATOMIC_RELAXED, __HIP_MEMORY_SCOPE_AGENT); WAITV(0); if (lane == 0) raise(cnt + (C_GB + sp) * 32, tag); } }
    DW4();
    asm volatile("s_waitcnt vmcnt(0)\n\ts_barrier" ::: "memory");
  }
  STW(12);
  {
    IDS();
    const char* ap = actfm + ((size_t)((tn & 1) * 62 + wk) * 128 + tm * 8 + wm * 2) * 1024;
    const char* w2 = wdt + (size_t)(tn * 448 + 200 + w) * 1024;
    const char* a2 = actfm + ((size_t)(50 + wk) * 128 + tm * 8 + wm * 2) * 1024;
    const unsigned va0 = vlin + wk * 1024, va1 = vlin + (wk ^ 1) * 1024;
    u32x4 ar[4][4];
#define DA(u, k) asm volatile("global_load_dwordx4 %0, %1, %2 sc1" : "=v"(ar[u][k]) : "v"((k) ? va1 : va0), "s"(ap) : "memory")
#define D2W(u, j, h) glds16c(w2 + ((j) * 16 + (h) * 8) * 1024, vlin, lds0 + (u) * 16384 + (h) * 8192 + w * 1024)
#define D2A(u, j, k) GLOAD4C(ar[u][k], a2 + (size_t)((j) * 4 + ((k) >> 1) * 2) * 131072, ((k) & 1) ? va1 : va0)
#define DNEXT() do { wp += 8192; ap += 262144; } while (0)
    f32x4 acc[2][8] = {};
    lds_cptr a6 = L + wk * 4096 + lane * 16; PIN(a6);
#pragma unroll
    for (int t = 0; t < 8; ++t) PIN2(acc[0][t], acc[1][t]);
    bf16x8 wf[2][8];
#define DFR(u, t) wf[(u) & 1][t] = ld128(a6 + (u) * 16384 + ((t) >> 2) * 8192 + ((t) & 3) * 1024)
#define DMM(u, CM, NM, t) do { if (NM) DFR(((u) + 1) & 3, t); SBAR(); constexpr int c_ = (CM) == 1 ? (t) : ((t) & 3), h_ = (CM) == 1 ? 0 : 2 * ((t) >> 2); \
    acc[0][c_] = MFMA16(wf[(u) & 1][t], B8(ar[u][h_]), acc[0][c_]); acc[1][c_] = MFMA16(wf[(u) & 1][t], B8(ar[u][h_ + 1]), acc[1][c_]); PIN2(acc[0][c_], acc[1][c_]); SBAR(); } while (0)
#define DSTEP(u, N, CM, NM, PRE, L0, L1, L2, L3) do { \
    if ((CM) == 1) asm volatile("s_waitcnt vmcnt(" #N ") lgkmcnt(0)\n\ts_barrier" : "+v"(ar[u][0]), "+v"(ar[u][1]) :: "memory"); \
    else asm volatile("s_waitcnt vmcnt(" #N ") lgkmcnt(0)\n\ts_barrier" : "+v"(ar[u][0]), "+v"(ar[u][1]), "+v"(ar[u][2]), "+v"(ar[u][3]) :: "memory"); \
    PRE; SBAR(); DMM(u, CM, NM, 0); L0; DMM(u, CM, NM, 1); DMM(u, CM, NM, 2); L1; DMM(u, CM, NM, 3); DMM(u, CM, NM, 4); L2; DMM(u, CM, NM, 5); DMM(u, CM, NM, 6); L3; DMM(u, CM, NM, 7); } while (0)
#define DSTD(u, N) do { DNEXT(); DSTEP(u, N, 1, 1, (void)0, DW(u, 0), DW(u, 1), DA(((u) + 3) & 3, 0), DA(((u) + 3) & 3, 1)); } while (0)
#pragma unroll
    for (int t = 0; t < 8; ++t) DFR(0, t);
    if (w == 0) await_n<2>(cnt + (C_ACT + (tm >> 1) * 32 + (tn & 1) * 12) * 32, 16, tag); else if (w == 1) await_n<2>(cnt + (C_GB + tn * 2) * 32, 2, tag);
    BAR();
    STW(13);
    glds16c(ws + WS_GBF + tn * 512, (unsigned)((lane & 31) * 16), lds0 + R4 + w * 1024);
    DA(0, 0); DA(0, 1); ap += 262144; DA(1, 0); DA(1, 1); ap += 262144; DA(2, 0); DA(2, 1);
    DSTD(0, 4); DSTD(1, 6); DSTD(2, 8); DSTD(3, 8);
#pragma unroll 1
    for (int s = 4; s < 20; s += 4) { DSTD(0, 8); DSTD(1, 8); DSTD(2, 8); DSTD(3, 8); }
    lds_ptr xb = (lds_ptr)lds + R2;
    const int pb = blockIdx.x ^ (MAP2 ? 16 : 32);
    char* pmine = ws + WS_PART + (size_t)(blockIdx.x * 8 + w) * 2048; const char* ptheir = ws + WS_PART + (size_t)(pb * 8 + w) * 2048;
#define DSEND() do { f32x4 sn[4]; _Pragma("unroll") for (int t = 0; t < 4; ++t) sn[t] = *(const LAS f32x4*)(xb + 32768 + (w ^ 4) * 4096 + t * 1024 + lane * 16) + acc[0][4 + t]; \
      _Pragma("unroll") for (int k = 0; k < 2; ++k) store16(pmine + k * 1024 + lane * 16, (u32x4){cvtpk(sn[2 * k][0], sn[2 * k][1]), cvtpk(sn[2 * k][2], sn[2 * k][3]), cvtpk(sn[2 * k + 1][0], sn[2 * k + 1][1]), cvtpk(sn[2 * k + 1][2], sn[2 * k + 1][3])}); } while (0)
    DSTD(0, 8);
    DNEXT(); DSTEP(1, 8, 1, 1, (void)0, D2W(1, 0, 0), D2W(1, 0, 1), DA(0, 0), DA(0, 1));
    DSTEP(2, 8, 1, 1, (void)0, D2W(2, 1, 0); D2W(2, 1, 1), D2A(1, 0, 0); D2A(1, 0, 1), D2A(1, 0, 2), D2A(1, 0, 3));
    DSTEP(3, 10, 1, 1, (void)0, D2W(3, 2, 0); D2W(3, 2, 1), D2A(2, 1, 0); D2A(2, 1, 1), D2A(2, 1, 2), D2A(2, 1, 3));
    DSTEP(0, 12, 1, 1, (void)0, D2A(3, 2, 0), D2A(3, 2, 1), D2A(3, 2, 2), D2A(3, 2, 3));
#pragma unroll
    for (int t = 0; t < 4; ++t) *(LAS f32x4*)(xb + 32768 + w * 4096 + t * 1024 + lane * 16) = acc[1][4 + t];
    DSTEP(1, 10, 2, 1, DSEND(), (void)0, (void)0, (void)0, (void)0);
    DSTEP(2, 6, 2, 1, (void)0, (void)0, (void)0, (void)0, (void)0);
    DSTEP(3, 0, 2, 0, if (tid == 0) raise(cnt + (C_PART + blockIdx.x) * 32, tag), (void)0, (void)0, (void)0, (void)0);
    STW(14);
    const int g = lane >> 4, tok = tm * 128 + wm * 32 + wk * 16 + (lane & 15);
#pragma unroll
    for (int t = 0; t < 4; ++t) *(LAS f32x4*)(xb + w * 4096 + t * 1024 + lane * 16) = acc[1][t];
    if (w == 1) await<1>(cnt + (C_PART + pb) * 32, tag);
    asm volatile("s_waitcnt lgkmcnt(0)\n\ts_barrier" ::: "memory");
    u32x4 rc[2];
    GLOAD4C(rc[0], ptheir, vlin); GLOAD4C(rc[1], ptheir + 1024, vlin);
    f32x4 o[4], gb[4];
#pragma unroll
    for (int t = 0; t < 4; ++t) { gb[t] = rbf4(*(const LAS f32x4*)(L + R4 + w * 1024 + t * 64 + g * 16) + *(const LAS f32x4*)(L + R4 + w * 1024 + 256 + t * 64 + g * 16));
      const f32x4 xv = {bflo(x1p[t >> 1][2 * (t & 1)]), bfhi(x1p[t >> 1][2 * (t & 1)]), bflo(x1p[t >> 1][2 * (t & 1) + 1]), bfhi(x1p[t >> 1][2 * (t & 1) + 1])};
      o[t] = xv + gb[t] * (*(const LAS f32x4*)(xb + (w ^ 4) * 4096 + t * 1024 + lane * 16) + acc[0][t]); PIN2(o[t], gb[t]); }
    asm volatile("s_waitcnt vmcnt(0)" : "+v"(rc[0]), "+v"(rc[1]) :: "memory");
#pragma unroll
    for (int jj = 0; jj < 2; ++jj) {
      unsigned pk[2][2];
#pragma unroll
      for (int tt = 0; tt < 2; ++tt) { const int t = 2 * jj + tt;
        const f32x4 p = {bflo(rc[jj][2 * tt]), bfhi(rc[jj][2 * tt]), bflo(rc[jj][2 * tt + 1]), bfhi(rc[jj][2 * tt + 1])}, r = o[t] + gb[t] * p;
        pk[tt][0] = cvtpk(r[0], r[1]); pk[tt][1] = cvtpk(r[2], r[3]); }
      auto r0 = __builtin_amdgcn_permlane16_swap(pk[0][0], pk[1][0], false, false);
      auto r1 = __builtin_amdgcn_permlane16_swap(pk[0][1], pk[1][1], false, false);
#define OUTWT 5
      bf16_t* op_ = out + (size_t)tok * D_ + tn * 64 + jj * 32 + (g & 1) * 16 + (g >> 1) * 8; const u32x4 ov_ = {r0[0], r1[0], r0[1], r1[1]};
      if (OUTWT == 1) store16(op_, ov_);
      else if (OUTWT == 2) __builtin_nontemporal_store(ov_, (u32x4*)op_);
      else if (OUTWT >= 3) *(LAS u32x4*)((lds_ptr)lds + w * 2048 + (lane & 15) * 128 + (((jj * 4 + (g & 1) * 2 + (g >> 1)) ^ (lane & 7)) << 4)) = ov_;
      else *(u32x4*)op_ = ov_;
    }
    if (OUTWT >= 3) {
#pragma unroll
      for (int k = 0; k < 2; ++k) { const u32x4 v_ = *(const LAS u32x4*)(L + w * 2048 + k * 1024 + (lane >> 3) * 128 + (((lane & 7) ^ (lane >> 3)) << 4));
        bf16_t* op_ = out + (size_t)(tm * 128 + wm * 32 + wk * 16 + k * 8 + (lane >> 3)) * D_ + tn * 64 + (lane & 7) * 8;
        if (OUTWT == 3) store16(op_, v_); else if (OUTWT == 4) *(u32x4*)op_ = v_; else __builtin_nontemporal_store(v_, (u32x4*)op_); }
    }
  }
}

extern "C" __global__ void k7_end() {}

extern "C" void kernel_launch(void* const* d_in, const int* in_sizes, int n_in, void* d_out, int out_size, void* d_ws, size_t ws_size, hipStream_t stream) {
  const bf16_t* x = (const bf16_t*)d_in[0]; const bf16_t* wq = (const bf16_t*)d_in[1]; const bf16_t* wk = (const bf16_t*)d_in[2];
  const bf16_t* wv = (const bf16_t*)d_in[3]; const bf16_t* wo = (const bf16_t*)d_in[4]; const bf16_t* wg = (const bf16_t*)d_in[6];
  const bf16_t* wu = (const bf16_t*)d_in[7]; const bf16_t* wd = (const bf16_t*)d_in[8]; const bf16_t* c = (const bf16_t*)d_in[9];
  const bf16_t* wmod = (const bf16_t*)d_in[10];
  char* ws = (char*)d_ws;
  (void)hipFuncSetAttribute((const void*)k3_main, hipFuncAttributeMaxDynamicSharedMemorySize, 163840);
  k3_main<<<256, 512, 163840, stream>>>(x, wq, wk, wv, wo, wg, wu, wd, c, wmod, ws, (bf16_t*)d_out STAMP_PASS(2));
}
```
